```python
import torch

def _softmax(x, axis=-1):
    return torch.softmax(x, dim=axis)
def _gelu(x):
    return torch.nn.functional.gelu(x, approximate="tanh")
def _silu(x):
    return torch.nn.functional.silu(x)
def _sigmoid(x):
    return torch.sigmoid(x)
def _layernorm(x, axis=-1, eps=1e-5):
    m = x.mean(dim=axis, keepdim=True)
    v = x.var(dim=axis, keepdim=True, unbiased=False)
    return (x - m) / torch.sqrt(v + eps)
def _scatter_add(tgt, idx, src, axis=0):
    return tgt.clone().index_add_(axis, idx, src)
def _segment_sum(src, seg, n):
    out = torch.zeros((n,) + src.shape[1:], dtype=src.dtype)
    return out.index_add_(0, seg, src)
def _pad(x, pw):
    flat = tuple(v for pair in reversed(pw) for v in pair)
    return torch.nn.functional.pad(x, flat)

kernel_name = 'gated_mix__grouped_kv_mqa__glu_gated_bilinear__prompt'

def setup_inputs():

    _w = (lambda a: (lambda t: t.to(torch.bfloat16) if t.is_floating_point() else t)(torch.as_tensor(a)))
    def _A_setup():
        B, S, D, H = 1, 2048, 512, 32
        Dh = D // H
        return {
            "x": _w((lambda *s: torch.randn(*s, dtype=torch.bfloat16) * 0.05)(B, S, D)),
            "wq": _w((lambda *s: torch.randn(*s, dtype=torch.bfloat16) * 0.8)(D, H * Dh)),
            "wk": _w((lambda *s: torch.randn(*s, dtype=torch.bfloat16) * 0.8)(D, Dh)),
            "wv": _w((lambda *s: torch.randn(*s, dtype=torch.bfloat16) * 0.05)(D, Dh)),
            "wo": _w((lambda *s: torch.randn(*s, dtype=torch.bfloat16) * 0.05)(H * Dh, D)),
        }
    _A_in = _A_setup()

    def _B_setup():
        B, S, D, F = 1, 2048, 512, 1792
        return {
            "x": _w((lambda *s: torch.randn(*s, dtype=torch.bfloat16) * 0.05)(B, S, D)),
            "w_gate": _w((lambda *s: torch.randn(*s, dtype=torch.bfloat16) * 0.8)(D, F) * (D ** -0.5)),
            "w_up": _w((lambda *s: torch.randn(*s, dtype=torch.bfloat16) * 0.8)(D, F) * (D ** -0.5)),
            "w_down": _w((lambda *s: torch.randn(*s, dtype=torch.bfloat16) * 0.8)(F, D) * (F ** -0.5)),
        }
    _B_in = _B_setup()

    return {
        "A_x": _A_in["x"],
        "A_wq": _A_in["wq"],
        "A_wk": _A_in["wk"],
        "A_wv": _A_in["wv"],
        "A_wo": _A_in["wo"],
        "B_x": _B_in["x"],
        "B_w_gate": _B_in["w_gate"],
        "B_w_up": _B_in["w_up"],
        "B_w_down": _B_in["w_down"],
        "gate_w": _w((lambda *s: torch.randn(*s, dtype=torch.bfloat16) * 0.05)(512) * 16.0),
    }

def reference(A_x, A_wq, A_wk, A_wv, A_wo, B_x, B_w_gate, B_w_up, B_w_down, gate_w):
    def _A_ref(x, wq, wk, wv, wo):
        B, S, D = x.shape
        H = 32
        Dh = D // H
        q = (lambda x, *d: x.permute(*d))((x @ wq).reshape(B, S, H, Dh), 0, 2, 1, 3)
        k = x @ wk
        v = x @ wv
        scores = torch.einsum("bhqd,bkd->bhqk", q, k) * (Dh ** -0.5)
        p = _softmax(scores, axis=-1)
        o = torch.einsum("bhqk,bkd->bhqd", p, v)
        return (lambda x, *d: x.permute(*d))(o, 0, 2, 1, 3).reshape(B, S, H * Dh) @ wo

    def _B_ref(x, w_gate, w_up, w_down):
        g = torch.einsum("bsd,df->bsf", x, w_gate)
        u = torch.einsum("bsd,df->bsf", x, w_up)
        h = g * u
        return torch.einsum("bsf,fd->bsd", h, w_down)

    x = A_x
    g = _sigmoid((x * gate_w).sum(-1, keepdims=True))
    a = _A_ref(x, A_wq, A_wk, A_wv, A_wo)
    b = _B_ref(x, B_w_gate, B_w_up, B_w_down)
    return g * a + (1 - g) * b
```

```cpp
#include <hip/hip_runtime.h>
#include <hip/hip_bf16.h>
#include <stdint.h>

typedef __attribute__((ext_vector_type(8))) __bf16 bf16x8;
typedef __attribute__((ext_vector_type(16))) __bf16 bf16x16;
typedef __attribute__((ext_vector_type(16))) float f32x16;
typedef __attribute__((ext_vector_type(8))) float f32x8;
typedef __attribute__((ext_vector_type(4))) float f32x4;
typedef __attribute__((ext_vector_type(4))) unsigned u32x4;
typedef __attribute__((ext_vector_type(2))) unsigned u32x2;
typedef short v4i16_t __attribute__((ext_vector_type(4)));
typedef short v8i16_t __attribute__((ext_vector_type(8)));
typedef unsigned short u16;
#define LAS __attribute__((address_space(3)))
typedef LAS const char* lds_cptr;

constexpr int S_ = 2048, D_ = 512, F_ = 1792, NH = 32;
constexpr int KB_H = F_ / 16;
constexpr int KB_ALL = KB_H + NH;
constexpr float QSCALE = 0.25f * 1.4426950408889634f;

constexpr size_t WS_AF = 0;
constexpr size_t WS_WF = WS_AF + (size_t)32 * KB_ALL * 2048;
constexpr size_t WS_QF = WS_WF + (size_t)8 * KB_ALL * 2048;
constexpr size_t WS_KV = WS_QF + (size_t)64 * 32 * 1024;
constexpr int KVREC = 2080, KVIMG = 64 * KVREC;
constexpr size_t WS_G = WS_KV + KVIMG;
constexpr size_t WS_KMAX = WS_G + 2048 * 4;

#define MFMA32(a, b, c) __builtin_amdgcn_mfma_f32_32x32x16_bf16(a, b, c, 0, 0, 0)
#define SBAR() __builtin_amdgcn_sched_barrier(0)
#define PIN(x) asm volatile("" : "+v"(x))

__device__ __forceinline__ void glds16(const void* g, unsigned lds_base) {
    unsigned sv;
    lds_base = __builtin_amdgcn_readfirstlane(lds_base);
    asm volatile("s_mov_b32 %0, m0\n\ts_mov_b32 m0, %2\n\ts_nop 0\n\tglobal_load_lds_dwordx4 %1, off\n\ts_mov_b32 m0, %0"
                 : "=&s"(sv) : "v"(g), "s"(lds_base) : "memory");
}
__device__ __forceinline__ unsigned cvtpk(float lo, float hi) {
    typedef __attribute__((ext_vector_type(2))) __bf16 bf16x2;
    typedef __attribute__((ext_vector_type(2))) float f32x2;
    f32x2 v = {lo, hi};
    return __builtin_bit_cast(unsigned, __builtin_convertvector(v, bf16x2));
}
__device__ __forceinline__ float bf_lo(unsigned u) { return __uint_as_float(u << 16); }
__device__ __forceinline__ float bf_hi(unsigned u) { return __uint_as_float(u & 0xffff0000u); }
__device__ __forceinline__ bf16x8 lds_frag(lds_cptr p) { return *(const LAS bf16x8*)p; }
__device__ __forceinline__ v4i16_t lds_tr(lds_cptr p) { return __builtin_amdgcn_ds_read_tr16_b64_v4i16((LAS v4i16_t*)p); }
__device__ __forceinline__ bf16x8 tr_frag(lds_cptr p0, lds_cptr p1) {
    v4i16_t a = lds_tr(p0), b = lds_tr(p1);
    v8i16_t r = {a[0], a[1], a[2], a[3], b[0], b[1], b[2], b[3]};
    return __builtin_bit_cast(bf16x8, r);
}
__device__ __forceinline__ float xhalf_sum(float v) {
    auto rr = __builtin_amdgcn_permlane32_swap(__float_as_uint(v), __float_as_uint(v), false, false);
    return __uint_as_float(rr[0]) + __uint_as_float(rr[1]);
}
__device__ __forceinline__ float xhalf_max(float v) {
    auto rr = __builtin_amdgcn_permlane32_swap(__float_as_uint(v), __float_as_uint(v), false, false);
    return __builtin_fmaxf(__uint_as_float(rr[0]), __uint_as_float(rr[1]));
}

constexpr int K1_XB = 16384, K1_WB = 32768, K1_KVB = 4096, K1_STAGE = K1_XB + K1_WB + K1_KVB;
constexpr int K1_LDS = 3 * K1_STAGE;

template <int MODE>
__device__ __forceinline__ void k1_main(const u16* __restrict__ x, const u16* __restrict__ wq, const u16* __restrict__ wk,
                                        const u16* __restrict__ wv, const u16* __restrict__ wg, const u16* __restrict__ wu,
                                        char* __restrict__ ws, char* lds, int tt, int ct, int wid, int lane) {
    constexpr bool KV = (MODE == 1);
    const unsigned lds0 = (unsigned)(uintptr_t)lds;
    const int r32 = lane & 31, hi = lane >> 5;
    const int th = wid & 1, cq = wid >> 1;
    const u16* xs[2];
#pragma unroll
    for (int i = 0; i < 2; ++i) {
        const int row = 8 * (2 * wid + i) + (lane >> 3);
        xs[i] = x + (size_t)(tt * 128 + row) * D_ + (((lane & 7) ^ ((row >> 1) & 7)) << 3);
    }
    const u16* wsrc[4];
    constexpr int WLD = (MODE == 2) ? D_ : F_;
#pragma unroll
    for (int i = 0; i < 4; ++i) {
        const int kr = 2 * (4 * wid + i) + hi;
        const int cp = lane & 31, b = (cp >> 2) ^ (kr & 3), sel = (cp >> 1) & 1, half = cp & 1;
        if (MODE == 2) wsrc[i] = wq + (size_t)kr * D_ + 256 * (ct - 14) + 32 * b + 16 * sel + 8 * half;
        else wsrc[i] = (sel ? wu : wg) + (size_t)kr * F_ + 128 * ct + 16 * b + 8 * half;
    }
    const u16* kvsrc = nullptr;
    if (KV) { const int kr = 8 * wid + ((lane & 31) >> 2); kvsrc = (((lane >> 1) & 1) ? wv : wk) + kr * 16 + 8 * (lane & 1); }
    auto stage = [&](int c, int slot) {
        const unsigned sb = lds0 + slot * K1_STAGE;
#pragma unroll
        for (int i = 0; i < 2; ++i) glds16(xs[i] + c * 64, sb + (2 * wid + i) * 1024);
#pragma unroll
        for (int i = 0; i < 4; ++i) glds16(wsrc[i] + (size_t)c * 64 * WLD, sb + K1_XB + (4 * wid + i) * 1024);
        if (KV) { if (lane < 32) glds16(kvsrc + c * 64 * 16, sb + K1_XB + K1_WB + wid * 512); }
    };
    stage(0, 0); stage(1, 1); stage(2, 2);
    int xoff[4];
    {
        const int row = 64 * th + r32;
#pragma unroll
        for (int ks = 0; ks < 4; ++ks) xoff[ks] = row * 128 + (((2 * ks + hi) ^ ((row >> 1) & 7)) << 4);
    }
    int woff[2];
    const int q4 = (lane & 15) >> 2, p4 = lane & 3, sel = (lane >> 4) & 1;
#pragma unroll
    for (int e = 0; e < 2; ++e) woff[e] = K1_XB + (8 * hi + q4) * 512 + ((((2 * cq + e) ^ q4) * 4 + sel * 2 + (p4 >> 1)) << 4) + (p4 & 1) * 8;
    const int kvoff = K1_XB + K1_WB + (8 * hi + q4) * 64 + sel * 32 + p4 * 8;
    f32x16 acc[2][2] = {};
    f32x16 acckv = {};
    const bool dokv = KV && (cq < 2);
#pragma unroll
    for (int c = 0; c < 8; ++c) {
        const int slot = c % 3;
        if (c == 0) { if (KV) asm volatile("s_waitcnt vmcnt(14)\n\ts_barrier" ::: "memory"); else asm volatile("s_waitcnt vmcnt(12)\n\ts_barrier" ::: "memory"); }
        else if (c < 7) { if (KV) asm volatile("s_waitcnt vmcnt(7) lgkmcnt(0)\n\ts_barrier" ::: "memory"); else asm volatile("s_waitcnt vmcnt(6) lgkmcnt(0)\n\ts_barrier" ::: "memory"); }
        else asm volatile("s_waitcnt vmcnt(0) lgkmcnt(0)\n\ts_barrier" ::: "memory");
        if (c >= 1 && c + 2 < 8) stage(c + 2, (c + 2) % 3);
        const lds_cptr sb = (lds_cptr)lds + slot * K1_STAGE;
#pragma unroll
        for (int ks = 0; ks < 4; ++ks) {
            bf16x8 xf0 = lds_frag(sb + xoff[ks]), xf1 = lds_frag(sb + xoff[ks] + 4096);
            bf16x8 wf0 = tr_frag(sb + woff[0] + ks * 8192, sb + woff[0] + ks * 8192 + 2048);
            bf16x8 wf1 = tr_frag(sb + woff[1] + ks * 8192, sb + woff[1] + ks * 8192 + 2048);
            acc[0][0] = MFMA32(wf0, xf0, acc[0][0]);
            acc[0][1] = MFMA32(wf0, xf1, acc[0][1]);
            acc[1][0] = MFMA32(wf1, xf0, acc[1][0]);
            acc[1][1] = MFMA32(wf1, xf1, acc[1][1]);
            if (KV) {
                if (dokv) {
                    bf16x8 kf = tr_frag(sb + kvoff + ks * 1024, sb + kvoff + ks * 1024 + 256);
                    acckv = MFMA32(kf, cq ? xf1 : xf0, acckv);
                }
            }
        }
    }
    const int tp = 2 * tt + th;
    if (MODE == 2) {
#pragma unroll
        for (int e = 0; e < 2; ++e)
#pragma unroll
            for (int et = 0; et < 2; ++et) {
                const int head = (ct - 14) * 16 + 2 * (2 * cq + e);
                char* dst = ws + WS_QF + ((size_t)(2 * tp + et) * 32 + head) * 1024 + lane * 16;
                const f32x16 a = acc[e][et];
                u32x4 v0 = {cvtpk(a[0] * QSCALE, a[1] * QSCALE), cvtpk(a[2] * QSCALE, a[3] * QSCALE), cvtpk(a[4] * QSCALE, a[5] * QSCALE), cvtpk(a[6] * QSCALE, a[7] * QSCALE)};
                u32x4 v1 = {cvtpk(a[8] * QSCALE, a[9] * QSCALE), cvtpk(a[10] * QSCALE, a[11] * QSCALE), cvtpk(a[12] * QSCALE, a[13] * QSCALE), cvtpk(a[14] * QSCALE, a[15] * QSCALE)};
                *(u32x4*)dst = v0;
                *(u32x4*)(dst + 1024) = v1;
            }
    } else {
#pragma unroll
        for (int e = 0; e < 2; ++e)
#pragma unroll
            for (int et = 0; et < 2; ++et) {
                const int kb = ct * 8 + 2 * cq + e;
                char* dst = ws + WS_AF + (((size_t)tp * KB_ALL + kb) * 2 + et) * 1024 + lane * 16;
                const f32x16 a = acc[e][et];
                u32x4 v = {cvtpk(a[0] * a[8], a[1] * a[9]), cvtpk(a[2] * a[10], a[3] * a[11]), cvtpk(a[4] * a[12], a[5] * a[13]), cvtpk(a[6] * a[14], a[7] * a[15])};
                *(u32x4*)dst = v;
            }
        if (KV) {
            if (dokv) {
                const int tb = 2 * tp + cq;
                char* rec = ws + WS_KV + (size_t)tb * KVREC;
                u32x4 kv = {cvtpk(acckv[0], acckv[1]), cvtpk(acckv[2], acckv[3]), cvtpk(acckv[4], acckv[5]), cvtpk(acckv[6], acckv[7])};
                *(u32x4*)(rec + lane * 16) = kv;
                float n2 = 0.f;
#pragma unroll
                for (int i = 0; i < 4; ++i) { const float a = bf_lo(kv[i]), b = bf_hi(kv[i]); n2 += a * a + b * b; }
                n2 = xhalf_sum(n2);
#pragma unroll
                for (int o = 1; o < 32; o <<= 1) n2 = __builtin_fmaxf(n2, __shfl_xor(n2, o));
                if (lane == 0) ((float*)(ws + WS_KMAX))[tb] = n2;
                char* vrec = rec + 1024 + (r32 >> 4) * 528 + ((r32 >> 3) & 1) * 256 + (r32 & 7) * 2;
#pragma unroll
                for (int i = 0; i < 8; i += 2) {
                    const unsigned pk = cvtpk(acckv[8 + i], acckv[9 + i]);
                    const int d = (i & 3) + 8 * (i >> 2) + 4 * hi;
                    *(u16*)(vrec + d * 16) = (u16)(pk & 0xffffu);
                    *(u16*)(vrec + (d + 1) * 16) = (u16)(pk >> 16);
                }
                if (lane < 2) { u32x4 ones = {0x3f803f80u, 0x3f803f80u, 0x3f803f80u, 0x3f803f80u}; *(u32x4*)(rec + 1024 + 512 + lane * 528) = ones; }
            }
        }
    }
}

__global__ void __launch_bounds__(576) k1_proj(const u16* __restrict__ x, const u16* __restrict__ wq, const u16* __restrict__ wk,
                                               const u16* __restrict__ wv, const u16* __restrict__ wo, const u16* __restrict__ wg,
                                               const u16* __restrict__ wu, const u16* __restrict__ wd, const u16* __restrict__ gw,
                                               char* __restrict__ ws) {
    __shared__ __attribute__((aligned(16))) char lds[K1_LDS];
    const int b = blockIdx.x, xcd = b & 7, idx = b >> 3;
    const int lane = threadIdx.x & 63, wid = __builtin_amdgcn_readfirstlane(threadIdx.x >> 6);
    if (wid == 8) {
        const int r32 = lane & 31, hi = lane >> 5;
        u16 wv8[9][8];
#pragma unroll
        for (int i = 0; i < 9; ++i) {
            const int id = 9 * b + i, kb = id >> 4, nb = id & 15;
            const u16* src = (kb < KB_H ? wd + (size_t)kb * 16 * D_ : wo + (size_t)(kb - KB_H) * 16 * D_) + 32 * nb + r32 + 4 * hi * D_;
#pragma unroll
            for (int j = 0; j < 8; ++j) wv8[i][j] = src[(8 * (j >> 2) + (j & 3)) * D_];
        }
        const u32x4 gwv = *(const u32x4*)(gw + lane * 8);
        u32x4 xr[8];
#pragma unroll
        for (int t = 0; t < 8; ++t) xr[t] = *(const u32x4*)(x + (size_t)(8 * b + t) * D_ + lane * 8);
#pragma unroll
        for (int i = 0; i < 9; ++i) {
            const int id = 9 * b + i, kb = id >> 4, nb = id & 15;
            u32x4 v;
#pragma unroll
            for (int j = 0; j < 4; ++j) v[j] = (unsigned)wv8[i][2 * j] | ((unsigned)wv8[i][2 * j + 1] << 16);
            *(u32x4*)(ws + WS_WF + (((size_t)(nb >> 1) * KB_ALL + kb) * 2 + (nb & 1)) * 1024 + lane * 16) = v;
        }
        float gsel = 0.f;
#pragma unroll
        for (int t = 0; t < 8; ++t) {
            float s = 0.f;
#pragma unroll
            for (int j = 0; j < 4; ++j) s += bf_lo(xr[t][j]) * bf_lo(gwv[j]) + bf_hi(xr[t][j]) * bf_hi(gwv[j]);
#pragma unroll
            for (int o = 1; o < 64; o <<= 1) s += __shfl_xor(s, o);
            if (lane == t) gsel = s;
        }
        if (lane < 8) ((float*)(ws + WS_G))[8 * b + lane] = 1.0f / (1.0f + __expf(-gsel));
        return;
    }
    const int tt = 8 * (xcd & 1) + (idx & 7), ct = 4 * (xcd >> 1) + (idx >> 3);
    if (ct >= 14) k1_main<2>(x, wq, wk, wv, wg, wu, ws, lds, tt, ct, wid, lane);
    else if (ct == 13) k1_main<1>(x, wq, wk, wv, wg, wu, ws, lds, tt, ct, wid, lane);
    else k1_main<0>(x, wq, wk, wv, wg, wu, ws, lds, tt, ct, wid, lane);
}

#define SMF(a, p8, c) __builtin_amdgcn_smfmac_f32_32x32x32_bf16(a, __builtin_bit_cast(bf16x16, p8), c, 0xDDDDDDDD, 0, 0)
__device__ __forceinline__ f32x8 lo8(const f32x16& v) { return __builtin_shufflevector(v, v, 0, 1, 2, 3, 4, 5, 6, 7); }
__device__ __forceinline__ f32x8 hi8(const f32x16& v) { return __builtin_shufflevector(v, v, 8, 9, 10, 11, 12, 13, 14, 15); }

__global__ void __launch_bounds__(512) k2_attn(char* __restrict__ ws) {
    __shared__ __attribute__((aligned(16))) char lds[KVIMG];
    const int b = blockIdx.x, xcd = b & 7, idx = b >> 3;
    const int lane = threadIdx.x & 63, wid = __builtin_amdgcn_readfirstlane(threadIdx.x >> 6);
    const int r32 = lane & 31, hi = lane >> 5;
    const int tp = 4 * xcd + (idx >> 3), hg = idx & 7;
    const int et = wid & 1, head = 4 * hg + (wid >> 1);
    const unsigned lds0 = (unsigned)(uintptr_t)lds;
    const u32x4 qraw = *(const u32x4*)(ws + WS_QF + ((size_t)(2 * tp + et) * 32 + head) * 1024 + lane * 16);
    float kmax2 = ((const float*)(ws + WS_KMAX))[lane];
    const float gate = ((const float*)(ws + WS_G))[64 * tp + 32 * et + r32];
    for (int i = wid; i < KVIMG / 1024; i += 8) glds16(ws + WS_KV + (size_t)i * 1024 + lane * 16, lds0 + i * 1024);
#pragma unroll
    for (int o = 1; o < 64; o <<= 1) kmax2 = __builtin_fmaxf(kmax2, __shfl_xor(kmax2, o));
    float qn2 = 0.f;
#pragma unroll
    for (int i = 0; i < 4; ++i) { const float a = bf_lo(qraw[i]), c = bf_hi(qraw[i]); qn2 += a * a + c * c; }
    qn2 = xhalf_sum(qn2);
    const float bound = __builtin_sqrtf(qn2 * kmax2) * 1.001f + 1e-6f;
    const bool fast = __all(bound <= 40.0f);
    const bf16x8 qf = __builtin_bit_cast(bf16x8, qraw);
    asm volatile("s_waitcnt vmcnt(0)\n\ts_barrier" ::: "memory");
    const lds_cptr kp = (lds_cptr)lds + lane * 16;
    const lds_cptr vp = (lds_cptr)lds + 1024 + (r32 < 16 ? (hi * 16 + r32) * 16 : 512);
    f32x16 o = {};
    if (fast) {
        f32x16 cneg;
#pragma unroll
        for (int i = 0; i < 16; ++i) cneg[i] = -bound;
        PIN(cneg);
        f32x16 sA = MFMA32(lds_frag(kp), qf, cneg), sB;
        bf16x8 kn = lds_frag(kp + KVREC);
#define TILE(SC, SN, T, LAST)                                                                  \
        {                                                                                      \
            const bf16x8 v0 = lds_frag(vp + (T) * KVREC), v1 = lds_frag(vp + (T) * KVREC + 528); \
            if (!(LAST)) { SN = MFMA32(kn, qf, cneg); kn = lds_frag(kp + ((T) + 2) * KVREC); }   \
            _Pragma("unroll") for (int i = 0; i < 8; ++i) SC[i] = __builtin_amdgcn_exp2f(SC[i]); \
            o = SMF(v0, lo8(SC), o);                                                           \
            _Pragma("unroll") for (int i = 8; i < 16; ++i) SC[i] = __builtin_amdgcn_exp2f(SC[i]); \
            o = SMF(v1, hi8(SC), o);                                                           \
        }
        for (int t = 0; t < 60; t += 2) {
            TILE(sA, sB, t, false);
            TILE(sB, sA, t + 1, false);
        }
        TILE(sA, sB, 60, false);
        TILE(sB, sA, 61, false);
        { const bf16x8 v0 = lds_frag(vp + 62 * KVREC), v1 = lds_frag(vp + 62 * KVREC + 528);
          sB = MFMA32(kn, qf, cneg);
#pragma unroll
          for (int i = 0; i < 8; ++i) sA[i] = __builtin_amdgcn_exp2f(sA[i]);
          o = SMF(v0, lo8(sA), o);
#pragma unroll
          for (int i = 8; i < 16; ++i) sA[i] = __builtin_amdgcn_exp2f(sA[i]);
          o = SMF(v1, hi8(sA), o); }
        TILE(sB, sA, 63, true);
    } else {
        float m = -__builtin_inff();
        const f32x16 zero = {};
        for (int t = 0; t < 64; ++t) {
            f32x16 s = MFMA32(lds_frag(kp + t * KVREC), qf, zero);
            const bf16x8 v0 = lds_frag(vp + t * KVREC), v1 = lds_frag(vp + t * KVREC + 528);
            float tm = s[0];
#pragma unroll
            for (int i = 1; i < 16; ++i) tm = __builtin_fmaxf(tm, s[i]);
            tm = xhalf_max(tm);
            const float mn = __builtin_fmaxf(m, tm);
            const float alpha = __builtin_amdgcn_exp2f(m - mn);
            m = mn;
#pragma unroll
            for (int i = 0; i < 16; ++i) { o[i] *= alpha; s[i] = __builtin_amdgcn_exp2f(s[i] - mn); }
            o = SMF(v0, lo8(s), o);
            o = SMF(v1, hi8(s), o);
        }
    }
    const float sc = gate / o[8];
    u32x4 ov = {cvtpk(o[0] * sc, o[1] * sc), cvtpk(o[2] * sc, o[3] * sc), cvtpk(o[4] * sc, o[5] * sc), cvtpk(o[6] * sc, o[7] * sc)};
    *(u32x4*)(ws + WS_AF + (((size_t)tp * KB_ALL + KB_H + head) * 2 + et) * 1024 + lane * 16) = ov;
}

constexpr int RED_ROW = 272;
constexpr int RED_TILE = 64 * RED_ROW;
__global__ void __launch_bounds__(512) k3_out(const char* __restrict__ ws, u16* __restrict__ out) {
    __shared__ __attribute__((aligned(16))) char lds[8 * RED_TILE];
    const int b = blockIdx.x, xcd = b & 7, idx = b >> 3;
    const int lane = threadIdx.x & 63, wid = __builtin_amdgcn_readfirstlane(threadIdx.x >> 6);
    const int r32 = lane & 31, hi = lane >> 5;
    const int tp = 4 * xcd + (idx >> 3), np = idx & 7;
    const char* ap = ws + WS_AF + (size_t)tp * KB_ALL * 2048 + lane * 16;
    const char* wp = ws + WS_WF + (size_t)np * KB_ALL * 2048 + lane * 16;
    const float* gp = (const float*)(ws + WS_G) + 64 * tp + r32;
    const float om0 = 1.0f - gp[0], om1 = 1.0f - gp[32];
    f32x16 acc[2][2] = {};
    auto step = [&](int kb) {
        const bf16x8 a0 = *(const bf16x8*)(ap + (size_t)kb * 2048), a1 = *(const bf16x8*)(ap + (size_t)kb * 2048 + 1024);
        const bf16x8 w0 = *(const bf16x8*)(wp + (size_t)kb * 2048), w1 = *(const bf16x8*)(wp + (size_t)kb * 2048 + 1024);
        acc[0][0] = MFMA32(w0, a0, acc[0][0]);
        acc[0][1] = MFMA32(w0, a1, acc[0][1]);
        acc[1][0] = MFMA32(w1, a0, acc[1][0]);
        acc[1][1] = MFMA32(w1, a1, acc[1][1]);
    };
#pragma unroll
    for (int i = 0; i < 14; ++i) step(14 * wid + i);
#pragma unroll
    for (int i = 0; i < 16; ++i) { acc[0][0][i] *= om0; acc[1][0][i] *= om0; acc[0][1][i] *= om1; acc[1][1][i] *= om1; }
#pragma unroll
    for (int i = 0; i < 4; ++i) step(KB_H + 4 * wid + i);
    char* mine = lds + wid * RED_TILE;
#pragma unroll
    for (int en = 0; en < 2; ++en)
#pragma unroll
        for (int et = 0; et < 2; ++et)
#pragma unroll
            for (int g = 0; g < 4; ++g) {
                f32x4 v = {acc[en][et][4 * g], acc[en][et][4 * g + 1], acc[en][et][4 * g + 2], acc[en][et][4 * g + 3]};
                *(f32x4*)(mine + (32 * et + r32) * RED_ROW + (32 * en + 8 * g + 4 * hi) * 4) = v;
            }
    __syncthreads();
    const int tok = threadIdx.x >> 3, oc = threadIdx.x & 7;
    f32x4 s0 = {}, s1 = {};
#pragma unroll
    for (int w = 0; w < 8; ++w) {
        const char* p = lds + w * RED_TILE + tok * RED_ROW + oc * 32;
        s0 += *(const f32x4*)p; s1 += *(const f32x4*)(p + 16);
    }
    u32x4 v = {cvtpk(s0[0], s0[1]), cvtpk(s0[2], s0[3]), cvtpk(s1[0], s1[1]), cvtpk(s1[2], s1[3])};
    *(u32x4*)(out + (size_t)(64 * tp + tok) * D_ + 64 * np + 8 * oc) = v;
}

extern "C" void kernel_launch(void* const* d_in, const int* in_sizes, int n_in, void* d_out, int out_size, void* d_ws,
                              size_t ws_size, hipStream_t stream) {
    const u16* x = (const u16*)d_in[0];
    const u16* wq = (const u16*)d_in[1];
    const u16* wk = (const u16*)d_in[2];
    const u16* wv = (const u16*)d_in[3];
    const u16* wo = (const u16*)d_in[4];
    const u16* wg = (const u16*)d_in[6];
    const u16* wu = (const u16*)d_in[7];
    const u16* wd = (const u16*)d_in[8];
    const u16* gw = (const u16*)d_in[9];
    char* ws = (char*)d_ws;
    k1_proj<<<256, 576, 0, stream>>>(x, wq, wk, wv, wo, wg, wu, wd, gw, ws);
    k2_attn<<<256, 512, 0, stream>>>(ws);
    k3_out<<<256, 512, 0, stream>>>(ws, (u16*)d_out);
}
```

```cpp
#include <hip/hip_runtime.h>
#include <hip/hip_bf16.h>
#include <stdint.h>

typedef __attribute__((ext_vector_type(8))) __bf16 bf16x8;
typedef __attribute__((ext_vector_type(16))) __bf16 bf16x16;
typedef __attribute__((ext_vector_type(16))) float f32x16;
typedef __attribute__((ext_vector_type(8))) float f32x8;
typedef __attribute__((ext_vector_type(4))) float f32x4;
typedef __attribute__((ext_vector_type(4))) unsigned u32x4;
typedef __attribute__((ext_vector_type(2))) unsigned u32x2;
typedef short v4i16_t __attribute__((ext_vector_type(4)));
typedef short v8i16_t __attribute__((ext_vector_type(8)));
typedef unsigned short u16;
#define LAS __attribute__((address_space(3)))
typedef LAS const char* lds_cptr;

constexpr int S_ = 2048, D_ = 512, F_ = 1792, NH = 32;
constexpr int KB_H = F_ / 16;
constexpr int KB_ALL = KB_H + NH;
constexpr float QSCALE = 0.25f * 1.4426950408889634f;

constexpr size_t WS_AF = 0;
constexpr size_t WS_WF = WS_AF + (size_t)32 * KB_ALL * 2048;
constexpr size_t WS_QF = WS_WF + (size_t)8 * KB_ALL * 2048;
constexpr size_t WS_KV = WS_QF + (size_t)64 * 32 * 1024;
constexpr int KVREC = 2080, KVIMG = 64 * KVREC;
constexpr size_t WS_G = WS_KV + KVIMG;
constexpr size_t WS_KMAX = WS_G + 2048 * 4;

#define STAMP(kern, i)
#define MFMA32(a, b, c) __builtin_amdgcn_mfma_f32_32x32x16_bf16(a, b, c, 0, 0, 0)
#define SBAR() __builtin_amdgcn_sched_barrier(0)
#define PIN(x) asm volatile("" : "+v"(x))

__device__ __forceinline__ void glds16(const void* g, unsigned lds_base) {
    unsigned sv;
    lds_base = __builtin_amdgcn_readfirstlane(lds_base);
    asm volatile("s_mov_b32 %0, m0\n\ts_mov_b32 m0, %2\n\ts_nop 0\n\tglobal_load_lds_dwordx4 %1, off\n\ts_mov_b32 m0, %0"
                 : "=&s"(sv) : "v"(g), "s"(lds_base) : "memory");
}
__device__ __forceinline__ unsigned cvtpk(float lo, float hi) {
    typedef __attribute__((ext_vector_type(2))) __bf16 bf16x2;
    typedef __attribute__((ext_vector_type(2))) float f32x2;
    f32x2 v = {lo, hi};
    return __builtin_bit_cast(unsigned, __builtin_convertvector(v, bf16x2));
}
__device__ __forceinline__ float bf_lo(unsigned u) { return __uint_as_float(u << 16); }
__device__ __forceinline__ float bf_hi(unsigned u) { return __uint_as_float(u & 0xffff0000u); }
__device__ __forceinline__ bf16x8 lds_frag(lds_cptr p) { return *(const LAS bf16x8*)p; }
__device__ __forceinline__ v4i16_t lds_tr(lds_cptr p) { return __builtin_amdgcn_ds_read_tr16_b64_v4i16((LAS v4i16_t*)p); }
__device__ __forceinline__ bf16x8 tr_frag(lds_cptr p0, lds_cptr p1) {
    v4i16_t a = lds_tr(p0), b = lds_tr(p1);
    v8i16_t r = {a[0], a[1], a[2], a[3], b[0], b[1], b[2], b[3]};
    return __builtin_bit_cast(bf16x8, r);
}
__device__ __forceinline__ float xhalf_sum(float v) {
    auto rr = __builtin_amdgcn_permlane32_swap(__float_as_uint(v), __float_as_uint(v), false, false);
    return __uint_as_float(rr[0]) + __uint_as_float(rr[1]);
}
__device__ __forceinline__ float xhalf_max(float v) {
    auto rr = __builtin_amdgcn_permlane32_swap(__float_as_uint(v), __float_as_uint(v), false, false);
    return __builtin_fmaxf(__uint_as_float(rr[0]), __uint_as_float(rr[1]));
}

constexpr int K1_XB = 16384, K1_WB = 32768, K1_KVB = 4096, K1_STAGE = K1_XB + K1_WB + K1_KVB;
constexpr int K1_LDS = 3 * K1_STAGE;

template <int MODE>
__device__ __forceinline__ void k1_main(const u16* __restrict__ x, const u16* __restrict__ wq, const u16* __restrict__ wk,
                                        const u16* __restrict__ wv, const u16* __restrict__ wg, const u16* __restrict__ wu,
                                        char* __restrict__ ws, char* lds, int tt, int ct, int wid, int lane) {
    constexpr bool KV = (MODE == 1);
    const unsigned lds0 = (unsigned)(uintptr_t)lds;
    const int r32 = lane & 31, hi = lane >> 5;
    const int th = wid & 1, cq = wid >> 1;
    const u16* xs[2];
#pragma unroll
    for (int i = 0; i < 2; ++i) {
        const int row = 8 * (2 * wid + i) + (lane >> 3);
        xs[i] = x + (size_t)(tt * 128 + row) * D_ + (((lane & 7) ^ ((row >> 1) & 7)) << 3);
    }
    const u16* wsrc[4];
    constexpr int WLD = (MODE == 2) ? D_ : F_;
#pragma unroll
    for (int i = 0; i < 4; ++i) {
        const int kr = 2 * (4 * wid + i) + hi;
        const int cp = lane & 31, b = (cp >> 2) ^ (kr & 3), sel = (cp >> 1) & 1, half = cp & 1;
        if (MODE == 2) wsrc[i] = wq + (size_t)kr * D_ + 256 * (ct - 14) + 32 * b + 16 * sel + 8 * half;
        else wsrc[i] = (sel ? wu : wg) + (size_t)kr * F_ + 128 * ct + 16 * b + 8 * half;
    }
    const u16* kvsrc = nullptr;
    if (KV) { const int kr = 8 * wid + ((lane & 31) >> 2); kvsrc = (((lane >> 1) & 1) ? wv : wk) + kr * 16 + 8 * (lane & 1); }
    auto stage = [&](int c, int slot) {
        const unsigned sb = lds0 + slot * K1_STAGE;
#pragma unroll
        for (int i = 0; i < 2; ++i) glds16(xs[i] + c * 64, sb + (2 * wid + i) * 1024);
#pragma unroll
        for (int i = 0; i < 4; ++i) glds16(wsrc[i] + (size_t)c * 64 * WLD, sb + K1_XB + (4 * wid + i) * 1024);
        if (KV) { if (lane < 32) glds16(kvsrc + c * 64 * 16, sb + K1_XB + K1_WB + wid * 512); }
    };
    STAMP(0, 0);
    stage(0, 0); stage(1, 1); stage(2, 2);
    STAMP(0, 1);
    int xoff[4];
    {
        const int row = 64 * th + r32;
#pragma unroll
        for (int ks = 0; ks < 4; ++ks) xoff[ks] = row * 128 + (((2 * ks + hi) ^ ((row >> 1) & 7)) << 4);
    }
    int woff[2];
    const int q4 = (lane & 15) >> 2, p4 = lane & 3, sel = (lane >> 4) & 1;
#pragma unroll
    for (int e = 0; e < 2; ++e) woff[e] = K1_XB + (8 * hi + q4) * 512 + ((((2 * cq + e) ^ q4) * 4 + sel * 2 + (p4 >> 1)) << 4) + (p4 & 1) * 8;
    const int kvoff = K1_XB + K1_WB + (8 * hi + q4) * 64 + sel * 32 + p4 * 8;
    f32x16 acc[2][2] = {};
    f32x16 acckv = {};
    const bool dokv = KV && (cq < 2);
#pragma unroll
    for (int c = 0; c < 8; ++c) {
        const int slot = c % 3;
        if (c == 0) { if (KV) asm volatile("s_waitcnt vmcnt(14)\n\ts_barrier" ::: "memory"); else asm volatile("s_waitcnt vmcnt(12)\n\ts_barrier" ::: "memory"); }
        else if (c < 7) { if (KV) asm volatile("s_waitcnt vmcnt(7) lgkmcnt(0)\n\ts_barrier" ::: "memory"); else asm volatile("s_waitcnt vmcnt(6) lgkmcnt(0)\n\ts_barrier" ::: "memory"); }
        else asm volatile("s_waitcnt vmcnt(0) lgkmcnt(0)\n\ts_barrier" ::: "memory");
        STAMP(0, 2 + c);
        if (c >= 1 && c + 2 < 8) stage(c + 2, (c + 2) % 3);
        const lds_cptr sb = (lds_cptr)lds + slot * K1_STAGE;
#pragma unroll
        for (int ks = 0; ks < 4; ++ks) {
            bf16x8 xf0 = lds_frag(sb + xoff[ks]), xf1 = lds_frag(sb + xoff[ks] + 4096);
            bf16x8 wf0 = tr_frag(sb + woff[0] + ks * 8192, sb + woff[0] + ks * 8192 + 2048);
            bf16x8 wf1 = tr_frag(sb + woff[1] + ks * 8192, sb + woff[1] + ks * 8192 + 2048);
            acc[0][0] = MFMA32(wf0, xf0, acc[0][0]);
            acc[0][1] = MFMA32(wf0, xf1, acc[0][1]);
            acc[1][0] = MFMA32(wf1, xf0, acc[1][0]);
            acc[1][1] = MFMA32(wf1, xf1, acc[1][1]);
            if (KV) {
                if (dokv) {
                    bf16x8 kf = tr_frag(sb + kvoff + ks * 1024, sb + kvoff + ks * 1024 + 256);
                    acckv = MFMA32(kf, cq ? xf1 : xf0, acckv);
                }
            }
        }
    }
    STAMP(0, 10);
    const int tp = 2 * tt + th;
    if (MODE == 2) {
#pragma unroll
        for (int e = 0; e < 2; ++e)
#pragma unroll
            for (int et = 0; et < 2; ++et) {
                const int head = (ct - 14) * 16 + 2 * (2 * cq + e);
                char* dst = ws + WS_QF + ((size_t)(2 * tp + et) * 32 + head) * 1024 + lane * 16;
                const f32x16 a = acc[e][et];
                u32x4 v0 = {cvtpk(a[0] * QSCALE, a[1] * QSCALE), cvtpk(a[2] * QSCALE, a[3] * QSCALE), cvtpk(a[4] * QSCALE, a[5] * QSCALE), cvtpk(a[6] * QSCALE, a[7] * QSCALE)};
                u32x4 v1 = {cvtpk(a[8] * QSCALE, a[9] * QSCALE), cvtpk(a[10] * QSCALE, a[11] * QSCALE), cvtpk(a[12] * QSCALE, a[13] * QSCALE), cvtpk(a[14] * QSCALE, a[15] * QSCALE)};
                *(u32x4*)dst = v0;
                *(u32x4*)(dst + 1024) = v1;
            }
    } else {
#pragma unroll
        for (int e = 0; e < 2; ++e)
#pragma unroll
            for (int et = 0; et < 2; ++et) {
                const int kb = ct * 8 + 2 * cq + e;
                char* dst = ws + WS_AF + (((size_t)tp * KB_ALL + kb) * 2 + et) * 1024 + lane * 16;
                const f32x16 a = acc[e][et];
                u32x4 v = {cvtpk(a[0] * a[8], a[1] * a[9]), cvtpk(a[2] * a[10], a[3] * a[11]), cvtpk(a[4] * a[12], a[5] * a[13]), cvtpk(a[6] * a[14], a[7] * a[15])};
                *(u32x4*)dst = v;
            }
        if (KV) {
            if (dokv) {
                const int tb = 2 * tp + cq;
                char* rec = ws + WS_KV + (size_t)tb * KVREC;
                u32x4 kv = {cvtpk(acckv[0], acckv[1]), cvtpk(acckv[2], acckv[3]), cvtpk(acckv[4], acckv[5]), cvtpk(acckv[6], acckv[7])};
                *(u32x4*)(rec + lane * 16) = kv;
                float n2 = 0.f;
#pragma unroll
                for (int i = 0; i < 4; ++i) { const float a = bf_lo(kv[i]), b = bf_hi(kv[i]); n2 += a * a + b * b; }
                n2 = xhalf_sum(n2);
#pragma unroll
                for (int o = 1; o < 32; o <<= 1) n2 = __builtin_fmaxf(n2, __shfl_xor(n2, o));
                if (lane == 0) ((float*)(ws + WS_KMAX))[tb] = n2;
                char* vrec = rec + 1024 + (r32 >> 4) * 528 + ((r32 >> 3) & 1) * 256 + (r32 & 7) * 2;
#pragma unroll
                for (int i = 0; i < 8; i += 2) {
                    const unsigned pk = cvtpk(acckv[8 + i], acckv[9 + i]);
                    const int d = (i & 3) + 8 * (i >> 2) + 4 * hi;
                    *(u16*)(vrec + d * 16) = (u16)(pk & 0xffffu);
                    *(u16*)(vrec + (d + 1) * 16) = (u16)(pk >> 16);
                }
                if (lane < 2) { u32x4 ones = {0x3f803f80u, 0x3f803f80u, 0x3f803f80u, 0x3f803f80u}; *(u32x4*)(rec + 1024 + 512 + lane * 528) = ones; }
            }
        }
    }
    STAMP(0, 11);
}

__global__ void __launch_bounds__(576) k1_proj(const u16* __restrict__ x, const u16* __restrict__ wq, const u16* __restrict__ wk,
                                               const u16* __restrict__ wv, const u16* __restrict__ wo, const u16* __restrict__ wg,
                                               const u16* __restrict__ wu, const u16* __restrict__ wd, const u16* __restrict__ gw,
                                               char* __restrict__ ws) {
    __shared__ __attribute__((aligned(16))) char lds[K1_LDS];
    const int b = blockIdx.x, xcd = b & 7, idx = b >> 3;
    const int lane = threadIdx.x & 63, wid = __builtin_amdgcn_readfirstlane(threadIdx.x >> 6);
    if (wid == 8) {
        const int r32 = lane & 31, hi = lane >> 5;
        STAMP(0, 0);
        u16 wv8[9][8];
#pragma unroll
        for (int i = 0; i < 9; ++i) {
            const int id = 9 * b + i, kb = id >> 4, nb = id & 15;
            const u16* src = (kb < KB_H ? wd + (size_t)kb * 16 * D_ : wo + (size_t)(kb - KB_H) * 16 * D_) + 32 * nb + r32 + 4 * hi * D_;
#pragma unroll
            for (int j = 0; j < 8; ++j) wv8[i][j] = src[(8 * (j >> 2) + (j & 3)) * D_];
        }
        const u32x4 gwv = *(const u32x4*)(gw + lane * 8);
        u32x4 xr[8];
#pragma unroll
        for (int t = 0; t < 8; ++t) xr[t] = *(const u32x4*)(x + (size_t)(8 * b + t) * D_ + lane * 8);
#pragma unroll
        for (int i = 0; i < 9; ++i) {
            const int id = 9 * b + i, kb = id >> 4, nb = id & 15;
            u32x4 v;
#pragma unroll
            for (int j = 0; j < 4; ++j) v[j] = (unsigned)wv8[i][2 * j] | ((unsigned)wv8[i][2 * j + 1] << 16);
            *(u32x4*)(ws + WS_WF + (((size_t)(nb >> 1) * KB_ALL + kb) * 2 + (nb & 1)) * 1024 + lane * 16) = v;
        }
        float gsel = 0.f;
#pragma unroll
        for (int t = 0; t < 8; ++t) {
            float s = 0.f;
#pragma unroll
            for (int j = 0; j < 4; ++j) s += bf_lo(xr[t][j]) * bf_lo(gwv[j]) + bf_hi(xr[t][j]) * bf_hi(gwv[j]);
#pragma unroll
            for (int o = 1; o < 64; o <<= 1) s += __shfl_xor(s, o);
            if (lane == t) gsel = s;
        }
        if (lane < 8) ((float*)(ws + WS_G))[8 * b + lane] = 1.0f / (1.0f + __expf(-gsel));
        STAMP(0, 12);
        return;
    }
    const int tt = 8 * (xcd & 1) + (idx & 7), ct = 4 * (xcd >> 1) + (idx >> 3);
    if (ct >= 14) k1_main<2>(x, wq, wk, wv, wg, wu, ws, lds, tt, ct, wid, lane);
    else if (ct == 13) k1_main<1>(x, wq, wk, wv, wg, wu, ws, lds, tt, ct, wid, lane);
    else k1_main<0>(x, wq, wk, wv, wg, wu, ws, lds, tt, ct, wid, lane);
}

#define SMF(a, p8, c) __builtin_amdgcn_smfmac_f32_32x32x32_bf16(a, __builtin_bit_cast(bf16x16, p8), c, 0xDDDDDDDD, 0, 0)
__device__ __forceinline__ f32x8 lo8(const f32x16& v) { return __builtin_shufflevector(v, v, 0, 1, 2, 3, 4, 5, 6, 7); }
__device__ __forceinline__ f32x8 hi8(const f32x16& v) { return __builtin_shufflevector(v, v, 8, 9, 10, 11, 12, 13, 14, 15); }

__global__ void __launch_bounds__(512) k2_attn(char* __restrict__ ws) {
    __shared__ __attribute__((aligned(16))) char lds[KVIMG];
    const int b = blockIdx.x, xcd = b & 7, idx = b >> 3;
    const int lane = threadIdx.x & 63, wid = __builtin_amdgcn_readfirstlane(threadIdx.x >> 6);
    const int r32 = lane & 31, hi = lane >> 5;
    const int tp = 4 * xcd + (idx >> 3), hg = idx & 7;
    const int et = wid & 1, head = 4 * hg + (wid >> 1);
    const unsigned lds0 = (unsigned)(uintptr_t)lds;
    STAMP(1, 0);
    const u32x4 qraw = *(const u32x4*)(ws + WS_QF + ((size_t)(2 * tp + et) * 32 + head) * 1024 + lane * 16);
    float kmax2 = ((const float*)(ws + WS_KMAX))[lane];
    const float gate = ((const float*)(ws + WS_G))[64 * tp + 32 * et + r32];
    for (int i = wid; i < KVIMG / 1024; i += 8) glds16(ws + WS_KV + (size_t)i * 1024 + lane * 16, lds0 + i * 1024);
#pragma unroll
    for (int o = 1; o < 64; o <<= 1) kmax2 = __builtin_fmaxf(kmax2, __shfl_xor(kmax2, o));
    float qn2 = 0.f;
#pragma unroll
    for (int i = 0; i < 4; ++i) { const float a = bf_lo(qraw[i]), c = bf_hi(qraw[i]); qn2 += a * a + c * c; }
    qn2 = xhalf_sum(qn2);
    const float bound = __builtin_sqrtf(qn2 * kmax2) * 1.001f + 1e-6f;
    const bool fast = __all(bound <= 40.0f);
    const bf16x8 qf = __builtin_bit_cast(bf16x8, qraw);
    STAMP(1, 1);
    asm volatile("s_waitcnt vmcnt(0)\n\ts_barrier" ::: "memory");
    STAMP(1, 2);
    const lds_cptr kp = (lds_cptr)lds + lane * 16;
    const lds_cptr vp = (lds_cptr)lds + 1024 + (r32 < 16 ? (hi * 16 + r32) * 16 : 512);
    f32x16 o = {};
    if (fast) {
        f32x16 cneg;
#pragma unroll
        for (int i = 0; i < 16; ++i) cneg[i] = -bound;
        PIN(cneg);
        f32x16 sA = MFMA32(lds_frag(kp), qf, cneg), sB;
        bf16x8 kn = lds_frag(kp + KVREC);
#define TILE(SC, SN, T, LAST)                                                                  \
        {                                                                                      \
            const bf16x8 v0 = lds_frag(vp + (T) * KVREC), v1 = lds_frag(vp + (T) * KVREC + 528); \
            if (!(LAST)) { SN = MFMA32(kn, qf, cneg); kn = lds_frag(kp + ((T) + 2) * KVREC); }   \
            _Pragma("unroll") for (int i = 0; i < 8; ++i) SC[i] = __builtin_amdgcn_exp2f(SC[i]); \
            o = SMF(v0, lo8(SC), o);                                                           \
            _Pragma("unroll") for (int i = 8; i < 16; ++i) SC[i] = __builtin_amdgcn_exp2f(SC[i]); \
            o = SMF(v1, hi8(SC), o);                                                           \
        }
        for (int t = 0; t < 60; t += 2) {
            TILE(sA, sB, t, false);
            TILE(sB, sA, t + 1, false);
        }
        TILE(sA, sB, 60, false);
        TILE(sB, sA, 61, false);
        { const bf16x8 v0 = lds_frag(vp + 62 * KVREC), v1 = lds_frag(vp + 62 * KVREC + 528);
          sB = MFMA32(kn, qf, cneg);
#pragma unroll
          for (int i = 0; i < 8; ++i) sA[i] = __builtin_amdgcn_exp2f(sA[i]);
          o = SMF(v0, lo8(sA), o);
#pragma unroll
          for (int i = 8; i < 16; ++i) sA[i] = __builtin_amdgcn_exp2f(sA[i]);
          o = SMF(v1, hi8(sA), o); }
        TILE(sB, sA, 63, true);
    } else {
        float m = -__builtin_inff();
        const f32x16 zero = {};
        for (int t = 0; t < 64; ++t) {
            f32x16 s = MFMA32(lds_frag(kp + t * KVREC), qf, zero);
            const bf16x8 v0 = lds_frag(vp + t * KVREC), v1 = lds_frag(vp + t * KVREC + 528);
            float tm = s[0];
#pragma unroll
            for (int i = 1; i < 16; ++i) tm = __builtin_fmaxf(tm, s[i]);
            tm = xhalf_max(tm);
            const float mn = __builtin_fmaxf(m, tm);
            const float alpha = __builtin_amdgcn_exp2f(m - mn);
            m = mn;
#pragma unroll
            for (int i = 0; i < 16; ++i) { o[i] *= alpha; s[i] = __builtin_amdgcn_exp2f(s[i] - mn); }
            o = SMF(v0, lo8(s), o);
            o = SMF(v1, hi8(s), o);
        }
    }
    STAMP(1, 3);
    const float sc = gate / o[8];
    u32x4 ov = {cvtpk(o[0] * sc, o[1] * sc), cvtpk(o[2] * sc, o[3] * sc), cvtpk(o[4] * sc, o[5] * sc), cvtpk(o[6] * sc, o[7] * sc)};
    *(u32x4*)(ws + WS_AF + (((size_t)tp * KB_ALL + KB_H + head) * 2 + et) * 1024 + lane * 16) = ov;
    STAMP(1, 4);
}

constexpr int RED_ROW = 272;
constexpr int RED_TILE = 64 * RED_ROW;
__global__ void __launch_bounds__(512) k3_out(const char* __restrict__ ws, u16* __restrict__ out) {
    __shared__ __attribute__((aligned(16))) char lds[8 * RED_TILE];
    const int b = blockIdx.x, xcd = b & 7, idx = b >> 3;
    const int lane = threadIdx.x & 63, wid = __builtin_amdgcn_readfirstlane(threadIdx.x >> 6);
    const int r32 = lane & 31, hi = lane >> 5;
    const int tp = 4 * xcd + (idx >> 3), np = idx & 7;
    STAMP(2, 0);
    const char* ap = ws + WS_AF + (size_t)tp * KB_ALL * 2048 + lane * 16;
    const char* wp = ws + WS_WF + (size_t)np * KB_ALL * 2048 + lane * 16;
    const float* gp = (const float*)(ws + WS_G) + 64 * tp + r32;
    const float om0 = 1.0f - gp[0], om1 = 1.0f - gp[32];
    f32x16 acc[2][2] = {};
    auto step = [&](int kb) {
        const bf16x8 a0 = *(const bf16x8*)(ap + (size_t)kb * 2048), a1 = *(const bf16x8*)(ap + (size_t)kb * 2048 + 1024);
        const bf16x8 w0 = *(const bf16x8*)(wp + (size_t)kb * 2048), w1 = *(const bf16x8*)(wp + (size_t)kb * 2048 + 1024);
        acc[0][0] = MFMA32(w0, a0, acc[0][0]);
        acc[0][1] = MFMA32(w0, a1, acc[0][1]);
        acc[1][0] = MFMA32(w1, a0, acc[1][0]);
        acc[1][1] = MFMA32(w1, a1, acc[1][1]);
    };
#pragma unroll
    for (int i = 0; i < 14; ++i) step(14 * wid + i);
#pragma unroll
    for (int i = 0; i < 16; ++i) { acc[0][0][i] *= om0; acc[1][0][i] *= om0; acc[0][1][i] *= om1; acc[1][1][i] *= om1; }
#pragma unroll
    for (int i = 0; i < 4; ++i) step(KB_H + 4 * wid + i);
    STAMP(2, 1);
    char* mine = lds + wid * RED_TILE;
#pragma unroll
    for (int en = 0; en < 2; ++en)
#pragma unroll
        for (int et = 0; et < 2; ++et)
#pragma unroll
            for (int g = 0; g < 4; ++g) {
                f32x4 v = {acc[en][et][4 * g], acc[en][et][4 * g + 1], acc[en][et][4 * g + 2], acc[en][et][4 * g + 3]};
                *(f32x4*)(mine + (32 * et + r32) * RED_ROW + (32 * en + 8 * g + 4 * hi) * 4) = v;
            }
    __syncthreads();
    STAMP(2, 2);
    const int tok = threadIdx.x >> 3, oc = threadIdx.x & 7;
    f32x4 s0 = {}, s1 = {};
#pragma unroll
    for (int w = 0; w < 8; ++w) {
        const char* p = lds + w * RED_TILE + tok * RED_ROW + oc * 32;
        s0 += *(const f32x4*)p; s1 += *(const f32x4*)(p + 16);
    }
    u32x4 v = {cvtpk(s0[0], s0[1]), cvtpk(s0[2], s0[3]), cvtpk(s1[0], s1[1]), cvtpk(s1[2], s1[3])};
    *(u32x4*)(out + (size_t)(64 * tp + tok) * D_ + 64 * np + 8 * oc) = v;
    STAMP(2, 3);
}

extern "C" void kernel_launch(void* const* d_in, const int* in_sizes, int n_in, void* d_out, int out_size, void* d_ws,
                              size_t ws_size, hipStream_t stream) {
    const u16* x = (const u16*)d_in[0];
    const u16* wq = (const u16*)d_in[1];
    const u16* wk = (const u16*)d_in[2];
    const u16* wv = (const u16*)d_in[3];
    const u16* wo = (const u16*)d_in[4];
    const u16* wg = (const u16*)d_in[6];
    const u16* wu = (const u16*)d_in[7];
    const u16* wd = (const u16*)d_in[8];
    const u16* gw = (const u16*)d_in[9];
    char* ws = (char*)d_ws;
    k1_proj<<<256, 576, 0, stream>>>(x, wq, wk, wv, wo, wg, wu, wd, gw, ws);
    k2_attn<<<256, 512, 0, stream>>>(ws);
    k3_out<<<256, 512, 0, stream>>>(ws, (u16*)d_out);
}
```

```cpp
#include <hip/hip_runtime.h>
#include <hip/hip_bf16.h>
#include <stdint.h>

typedef __attribute__((ext_vector_type(2))) __bf16 bf16x2;
typedef __attribute__((ext_vector_type(8))) __bf16 bf16x8;
typedef __attribute__((ext_vector_type(16))) __bf16 bf16x16;
typedef __attribute__((ext_vector_type(16))) float f32x16;
typedef __attribute__((ext_vector_type(8))) float f32x8;
typedef __attribute__((ext_vector_type(4))) float f32x4;
typedef __attribute__((ext_vector_type(4))) unsigned u32x4;
typedef __attribute__((ext_vector_type(2))) unsigned u32x2;
typedef short v4i16_t __attribute__((ext_vector_type(4)));
typedef short v8i16_t __attribute__((ext_vector_type(8)));
typedef unsigned short u16;
#define LAS __attribute__((address_space(3)))
#define GAS __attribute__((address_space(1)))
typedef LAS const char* lds_cptr;
typedef LAS char* lds_ptr;
typedef LAS unsigned lu32;
typedef GAS unsigned gu32;
typedef GAS unsigned long long gu64;

constexpr int D_ = 512, F_ = 1792, NH = 32;
constexpr int KB_H = F_ / 16;
constexpr int KB_ALL = KB_H + NH;
constexpr float QSCALE = 0.25f * 1.4426950408889634f;

constexpr int WS_CHAL = 0, WS_ACK = 2048, WS_RESP = 4096;
constexpr int WS_FKV = 8192, WS_FH = 9216, WS_FW = 11264, WS_FO = 13312;
constexpr int WS_KMAX = 32768;
constexpr int KVREC = 2080, KVIMG = 64 * KVREC;
constexpr int WS_KV = 65536;
constexpr int WS_AF = 262144;
constexpr int WS_WF = WS_AF + 32 * KB_ALL * 2048;
constexpr int WS_END = WS_WF + 8 * KB_ALL * 2048;
constexpr int LEADER = 64;

constexpr int GB = 16384, UB = 32768, QB = 49152, STAGE = 53248, RING = 3 * STAGE;
constexpr int SP_FLAG = RING, SP_KMAX = RING + 256, SP_DUMP = RING + 512, SP_GW = RING + 1024, SP_G = RING + 2048, LDS_BYTES = RING + 4096;
constexpr int SP_Q = 0, KVOFF = 8192, SP_RT = KVOFF + KVIMG, SP_RED = 49152;
enum { FL_FULL = 32, FL_DONE = 8, FL_KVFULL = 16, FL_CHORE = 19, FL_GO = 20, FL_KVOK = 21, FL_GDONE = 22, FL_QDONE = 23, FL_HCNT = 24, FL_OCNT = 25, FL_KVRED = 27, FL_KMAX = 28, FL_KVSLOT = 29, FL_E = 30 };
constexpr int RED_ROW = 272, RED_TILE = 64 * RED_ROW;

#define STAMP(i)
#define AHEAD 1
#define PREFETCH 0
#define PSWITCH 2
#define ECHO0 12
#define ECHO1 16
#define ABL 0
#define MFMA32(a, b, c) __builtin_amdgcn_mfma_f32_32x32x16_bf16(a, b, c, 0, 0, 0)
#define SMF(a, p8, c) __builtin_amdgcn_smfmac_f32_32x32x32_bf16(a, __builtin_bit_cast(bf16x16, p8), c, 0xDDDDDDDD, 0, 0)
#define SBAR() __builtin_amdgcn_sched_barrier(0)
#define PIN(x) asm volatile("" : "+v"(x))
#define CBAR() asm volatile("" ::: "memory")
#define RLX_AGENT __ATOMIC_RELAXED, __HIP_MEMORY_SCOPE_AGENT
#define RLX_WG __ATOMIC_RELAXED, __HIP_MEMORY_SCOPE_WORKGROUP
#define VMCNT(n) asm volatile("s_waitcnt vmcnt(" #n ")" ::: "memory")
constexpr unsigned SPIN_CAP = 1u << 16;

__device__ __forceinline__ void glds16(const void* g, unsigned lds_base) {
    lds_base = __builtin_amdgcn_readfirstlane(lds_base);
    asm volatile("s_mov_b32 m0, %1\n\ts_nop 0\n\tglobal_load_lds_dwordx4 %0, off" :: "v"(g), "s"(lds_base) : "memory");
}
__device__ __forceinline__ void glds16s(const void* sbase, unsigned voff, unsigned lds_base) {
    asm volatile("s_mov_b32 m0, %2\n\ts_nop 0\n\tglobal_load_lds_dwordx4 %0, %1" :: "v"(voff), "s"(sbase), "s"(lds_base) : "memory");
}
__device__ __forceinline__ void glds16x4_sc1(const void* sbase, unsigned voff, unsigned lds_base) {
    asm volatile("s_mov_b32 m0, %2\n\ts_nop 0\n\tglobal_load_lds_dwordx4 %0, %1 sc1\n\tglobal_load_lds_dwordx4 %0, %1 offset:1024 sc1\n\t"
                 "global_load_lds_dwordx4 %0, %1 offset:2048 sc1\n\tglobal_load_lds_dwordx4 %0, %1 offset:3072 sc1" :: "v"(voff), "s"(sbase), "s"(lds_base) : "memory");
}
__device__ __forceinline__ void glds16x1_sc1(const void* sbase, unsigned voff, unsigned lds_base) {
    asm volatile("s_mov_b32 m0, %2\n\ts_nop 0\n\tglobal_load_lds_dwordx4 %0, %1 sc1" :: "v"(voff), "s"(sbase), "s"(lds_base) : "memory");
}
__device__ __forceinline__ unsigned cvtpk(float lo, float hi) {
    typedef __attribute__((ext_vector_type(2))) float f32x2;
    f32x2 v = {lo, hi};
    return __builtin_bit_cast(unsigned, __builtin_convertvector(v, bf16x2));
}
__device__ __forceinline__ u32x4 pack8(const f32x16& a, int o, float s) {
    return u32x4{cvtpk(a[o] * s, a[o + 1] * s), cvtpk(a[o + 2] * s, a[o + 3] * s), cvtpk(a[o + 4] * s, a[o + 5] * s), cvtpk(a[o + 6] * s, a[o + 7] * s)};
}
__device__ __forceinline__ u32x4 pack8n(const f32x16& a, int o) {
    return u32x4{cvtpk(a[o], a[o + 1]), cvtpk(a[o + 2], a[o + 3]), cvtpk(a[o + 4], a[o + 5]), cvtpk(a[o + 6], a[o + 7])};
}
__device__ __forceinline__ float bf_lo(unsigned u) { return __uint_as_float(u << 16); }
__device__ __forceinline__ float bf_hi(unsigned u) { return __uint_as_float(u & 0xffff0000u); }
__device__ __forceinline__ bf16x8 lds_frag(lds_cptr p) { return *(const LAS bf16x8*)p; }
__device__ __forceinline__ v4i16_t lds_tr(lds_cptr p) { return __builtin_amdgcn_ds_read_tr16_b64_v4i16((LAS v4i16_t*)p); }
__device__ __forceinline__ bf16x8 tr_frag(lds_cptr p0, lds_cptr p1) {
    v4i16_t a = lds_tr(p0), b = lds_tr(p1);
    v8i16_t r = {a[0], a[1], a[2], a[3], b[0], b[1], b[2], b[3]};
    return __builtin_bit_cast(bf16x8, r);
}
__device__ __forceinline__ float xhalf_sum(float v) {
    auto rr = __builtin_amdgcn_permlane32_swap(__float_as_uint(v), __float_as_uint(v), false, false);
    return __uint_as_float(rr[0]) + __uint_as_float(rr[1]);
}
__device__ __forceinline__ float xhalf_max(float v) {
    auto rr = __builtin_amdgcn_permlane32_swap(__float_as_uint(v), __float_as_uint(v), false, false);
    return __builtin_fmaxf(__uint_as_float(rr[0]), __uint_as_float(rr[1]));
}
__device__ __forceinline__ f32x8 lo8(const f32x16& v) { return __builtin_shufflevector(v, v, 0, 1, 2, 3, 4, 5, 6, 7); }
__device__ __forceinline__ f32x8 hi8(const f32x16& v) { return __builtin_shufflevector(v, v, 8, 9, 10, 11, 12, 13, 14, 15); }
__device__ __forceinline__ float norm2_8(const u32x4& v) {
    float n = 0.f;
#pragma unroll
    for (int i = 0; i < 4; ++i) { const float a = bf_lo(v[i]), b = bf_hi(v[i]); n += a * a + b * b; }
    return n;
}
__device__ __forceinline__ unsigned lds_ld(lu32* p) { return __hip_atomic_load(p, RLX_WG); }
__device__ __forceinline__ void lds_sig(lu32* p) {
    asm volatile("s_mov_b64 exec, 1\n\tds_add_u32 %0, %1\n\ts_mov_b64 exec, -1" :: "v"(p), "v"(1u) : "memory");
}
__device__ __forceinline__ void lds_sig2(lu32* p) {
    asm volatile("s_mov_b64 exec, 1\n\tds_add_u32 %0, %1\n\ts_mov_b64 exec, -1" :: "v"(p), "v"(2u) : "memory");
}
__device__ __forceinline__ unsigned lds_sig_ret(lu32* p) {
    unsigned r;
    asm volatile("s_mov_b64 exec, 1\n\tds_add_rtn_u32 %0, %1, %2\n\ts_waitcnt lgkmcnt(0)\n\ts_mov_b64 exec, -1" : "=&v"(r) : "v"(p), "v"(1u) : "memory");
    return __builtin_amdgcn_readfirstlane(r);
}
__device__ __forceinline__ void lds_wait(lu32* p, unsigned want) {
#pragma nounroll
    for (unsigned s = 0; s < SPIN_CAP; ++s) {
        CBAR();
        if ((unsigned)__builtin_amdgcn_readfirstlane(lds_ld(p)) >= want) break;
        __builtin_amdgcn_s_sleep(1);
    }
    CBAR();
}
template <int SLEEP>
__device__ __forceinline__ void glb_wait(gu64* p, unsigned long long E) {
#pragma nounroll
    for (unsigned s = 0; s < SPIN_CAP; ++s) {
        CBAR();
        if (__all(__hip_atomic_load(p, RLX_AGENT) == E)) break;
        __builtin_amdgcn_s_sleep(SLEEP);
    }
    CBAR();
}
__device__ __forceinline__ unsigned long long get_nonce(lu32* fl) {
    lds_wait(fl + FL_GO, 1);
    return (unsigned long long)lds_ld(fl + FL_E) | ((unsigned long long)lds_ld(fl + FL_E + 1) << 32);
}
#define FLAG_SET(off, E) do { if (lane == 0) __hip_atomic_store((gu64*)(ws + (off)), E, RLX_AGENT); } while (0)
#define ST16(v, off) __builtin_amdgcn_raw_buffer_store_b128(v, rs, off, 0, 16)
#define ST8(v, off) __builtin_amdgcn_raw_buffer_store_b64(v, rs, off, 0, 16)
#define LD16(off) __builtin_bit_cast(bf16x8, __builtin_amdgcn_raw_buffer_load_b128(rs, off, 0, 16))

__device__ __forceinline__ bool echo_round(const __amdgpu_buffer_rsrc_t rs, int lane, unsigned long long E) {
    const unsigned e0 = (unsigned)E, e1 = (unsigned)(E >> 32);
    CBAR();
    const u32x4 c0 = __builtin_amdgcn_raw_buffer_load_b128(rs, WS_CHAL + lane * 32, 0, 16), c1 = __builtin_amdgcn_raw_buffer_load_b128(rs, WS_CHAL + lane * 32 + 16, 0, 16);
    const u32x4 a0 = __builtin_amdgcn_raw_buffer_load_b128(rs, WS_ACK + lane * 32, 0, 16), a1 = __builtin_amdgcn_raw_buffer_load_b128(rs, WS_ACK + lane * 32 + 16, 0, 16);
    if (__all(a0[0] == e0 && a0[1] == e1 && a0[2] == e0 && a0[3] == e1 && a1[0] == e0 && a1[1] == e1 && a1[2] == e0 && a1[3] == e1)) return true;
    ST16((u32x4{c0[0] ^ e0, c0[1] ^ e1, e0, e1}), WS_RESP + lane * 64);
    ST16((u32x4{c0[2] ^ e0, c0[3] ^ e1, e0, e1}), WS_RESP + lane * 64 + 16);
    ST16((u32x4{c1[0] ^ e0, c1[1] ^ e1, e0, e1}), WS_RESP + lane * 64 + 32);
    ST16((u32x4{c1[2] ^ e0, c1[3] ^ e1, e0, e1}), WS_RESP + lane * 64 + 48);
    return false;
}

__global__ void __launch_bounds__(768) fused(const u16* __restrict__ x, const u16* __restrict__ wq, const u16* __restrict__ wk,
                                             const u16* __restrict__ wv, const u16* __restrict__ wo, const u16* __restrict__ wg,
                                             const u16* __restrict__ wu, const u16* __restrict__ wd, const u16* __restrict__ gw,
                                             char* ws, u16* __restrict__ out) {
    __shared__ __attribute__((aligned(16))) char lds[LDS_BYTES];
    const int b = blockIdx.x, xcd = b & 7, idx = b >> 3;
    const int lane = threadIdx.x & 63, wid = __builtin_amdgcn_readfirstlane(threadIdx.x >> 6);
    const int r32 = lane & 31, hi = lane >> 5;
    const int tt = 8 * (xcd & 1) + (idx & 7), ct = 4 * (xcd >> 1) + (idx >> 3);
#define ROTMODE 1
    const int rot = ROTMODE == 0 ? 0 : ROTMODE == 1 ? 4 * ((idx ^ (idx >> 3)) & 1) : ((idx & 7) + 2 * (idx >> 3)) & 7;
    const unsigned lds0 = (unsigned)(uintptr_t)lds;
    lu32* const fl = (lu32*)((lds_ptr)lds + SP_FLAG);
    const __amdgpu_buffer_rsrc_t rs = __builtin_amdgcn_make_buffer_rsrc(ws, 0, WS_END, 0x00020000);
    STAMP(0);
    if (wid == 0) fl[lane] = lane == FL_FULL + 8 ? 4u : 0u;
    if (wid < 8) {
        asm volatile("s_waitcnt lgkmcnt(0)\n\ts_barrier" ::: "memory");
        if (b == LEADER && wid >= 6) {
            const unsigned long long E = get_nonce(fl);
            __builtin_amdgcn_s_sleep(ECHO0);
            if (wid == 7) __builtin_amdgcn_s_sleep(ECHO1);
            echo_round(rs, lane, E);
        }
        if (PREFETCH && wid < 5) {
            const int id = idx * 64 + lane, kc = (3 + wid + rot) & 7, kr = kc * 64 + ((id >> 4) & 63), l = id & 15;
            const char* pa = id < 1024 ? (const char*)x + (size_t)(1024 * (xcd & 1) + id) * 1024 + kc * 128
                           : l < 14 ? (const char*)(l < 7 ? wg : wu) + (size_t)kr * (2 * F_) + 896 * (xcd >> 1) + 128 * (l < 7 ? l : l - 7)
                                    : (const char*)wq + (size_t)kr * (2 * D_) + 256 * (xcd >> 1) + 128 * (l - 14);
            asm volatile("s_mov_b32 m0, %1\n\ts_nop 0\n\tglobal_load_lds_dword %0, off\n\tglobal_load_lds_dword %0, off offset:64" :: "v"(pa), "s"(lds0 + SP_DUMP) : "memory");
        }
        if (PREFETCH && wid == 5) {
            const int rb = (((b & 31) * 9 + 1) >> 1) * 16;
#pragma unroll
            for (int i = 0; i < 2; ++i) {
                const int r = min(rb + 64 * i + lane, F_ + D_ - 1);
                const char* pa = (const char*)(r < F_ ? wd + (size_t)r * D_ : wo + (size_t)(r - F_) * D_) + 128 * (b >> 5);
                if (i == 0 || lane < 16)
                    asm volatile("s_mov_b32 m0, %1\n\ts_nop 0\n\tglobal_load_lds_dword %0, off\n\tglobal_load_lds_dword %0, off offset:64" :: "v"(pa), "s"(__builtin_amdgcn_readfirstlane(lds0 + SP_DUMP)) : "memory");
            }
        }
        const int th = wid & 1, cq = wid >> 1;
        int xoff[4], woff[2][4];
        {
            const int row = 64 * th + r32, q4 = (lane & 15) >> 2, p4 = lane & 3, sel = (lane >> 4) & 1;
#pragma unroll
            for (int j = 0; j < 4; ++j) {
                xoff[j] = row * 128 + (((2 * (cq ^ j) + hi) ^ ((row >> 1) & 7)) << 4);
#pragma unroll
                for (int e = 0; e < 2; ++e) {
                    const int ks = cq ^ j, L = 2 * cq + e;
                    woff[e][j] = L < 7 ? GB + sel * 16384 + (16 * ks + 8 * hi + q4) * 256 + ((L ^ (2 * q4) ^ sel) << 5) + p4 * 8
                                       : QB + (ks >> 1) * 2048 + (2 * (ks & 1) + hi) * 256 + q4 * 64 + sel * 32 + p4 * 8;
                }
            }
        }
        f32x16 acc[2][2] = {};
        bf16x8 xf[4][2]; v4i16_t wa[4][2], wb[4][2];
        unsigned ax, a0, a1;
#define ADDR(J) do { ax = so + xoff[J]; a0 = so + woff[0][J]; a1 = so + woff[1][J]; PIN(ax); PIN(a0); PIN(a1); } while (0)
#define LP(a) ((lds_cptr)(uintptr_t)(a))
#define WF(B, E) __builtin_bit_cast(bf16x8, (v8i16_t{wa[B][E][0], wa[B][E][1], wa[B][E][2], wa[B][E][3], wb[B][E][0], wb[B][E][1], wb[B][E][2], wb[B][E][3]}))
#define LOADALL(N) do { xf[N][0] = lds_frag(LP(ax)); wa[N][0] = lds_tr(LP(a0)); wb[N][0] = lds_tr(LP(a0) + 1024); xf[N][1] = lds_frag(LP(ax) + 4096); \
            wa[N][1] = lds_tr(LP(a1)); wb[N][1] = lds_tr(LP(a1) + 1024); } while (0)
#define STEP(B, N) do { SBAR();                                                                                                      \
            acc[0][0] = MFMA32(WF(B, 0), xf[B][0], acc[0][0]); SBAR(); xf[N][0] = lds_frag(LP(ax)); wa[N][0] = lds_tr(LP(a0)); SBAR();              \
            acc[0][1] = MFMA32(WF(B, 0), xf[B][1], acc[0][1]); SBAR(); wb[N][0] = lds_tr(LP(a0) + 1024); xf[N][1] = lds_frag(LP(ax) + 4096); SBAR(); \
            acc[1][0] = MFMA32(WF(B, 1), xf[B][0], acc[1][0]); SBAR(); wa[N][1] = lds_tr(LP(a1)); wb[N][1] = lds_tr(LP(a1) + 1024); SBAR();         \
            acc[1][1] = MFMA32(WF(B, 1), xf[B][1], acc[1][1]); SBAR(); } while (0)
        float ga0 = 0.f, ga1 = 0.f;
        const lds_cptr gwp = (lds_cptr)lds + SP_GW + cq * 32 + hi * 16;
        unsigned so = lds0 + STAGE;
        ADDR(0);
        lds_wait(fl + FL_FULL, 4);
        STAMP(1);
        u32x4 gv = *(const LAS u32x4*)(gwp + (rot & 7) * 128);
        LOADALL(0); ADDR(1); LOADALL(1); ADDR(2);
#pragma nounroll
        for (int c = 0; c < 8; ++c) {
            const unsigned nf = lds_ld(fl + FL_FULL + c + 1);
            STEP(0, 2);
            ADDR(3);
            {
                const u32x4 x0 = __builtin_bit_cast(u32x4, xf[0][0]), x1 = __builtin_bit_cast(u32x4, xf[0][1]);
#pragma unroll
                for (int j = 0; j < 4; ++j) {
                    const unsigned g_ = gv[j], a_ = x0[j], b_ = x1[j];
                    ga0 = __builtin_amdgcn_fdot2_f32_bf16(__builtin_bit_cast(bf16x2, a_), __builtin_bit_cast(bf16x2, g_), ga0, false);
                    ga1 = __builtin_amdgcn_fdot2_f32_bf16(__builtin_bit_cast(bf16x2, b_), __builtin_bit_cast(bf16x2, g_), ga1, false);
                }
                PIN(ga0); PIN(ga1);
            }
            STEP(1, 3);
            lds_sig(fl + FL_DONE + c);
            so = so == lds0 + 2 * STAGE ? lds0 : so + STAGE;
            ADDR(0);
            if (c == 7) {
                const float s0 = xhalf_sum(ga0), s1 = xhalf_sum(ga1);
                ((LAS float*)((lds_ptr)lds + SP_G))[cq * 128 + 64 * th + lane] = hi ? s1 : s0;
                lds_sig(fl + FL_GDONE);
            }
            if ((unsigned)__builtin_amdgcn_readfirstlane(nf) < 4u) lds_wait(fl + FL_FULL + c + 1, 4);
            CBAR();
            STEP(2, 0);
            ADDR(1);
            gv = *(const LAS u32x4*)(gwp + ((c + 1 + rot) & 7) * 128);
            STEP(3, 1);
            ADDR(2);
        }
        STAMP(2);
        if (cq == 3) {
            lds_wait(fl + FL_DONE + 5, 8);
#pragma unroll
            for (int et = 0; et < 2; ++et) {
                *(LAS u32x4*)((lds_ptr)lds + SP_Q + (2 * th + et) * 1024 + lane * 16) = pack8(acc[1][et], 0, QSCALE);
                *(LAS u32x4*)((lds_ptr)lds + SP_Q + 4096 + (2 * th + et) * 1024 + lane * 16) = pack8(acc[1][et], 8, QSCALE);
            }
            lds_sig(fl + FL_QDONE);
        }
        lds_wait(fl + FL_GDONE, 8);
        const LAS float* gl = (const LAS float*)((lds_ptr)lds + SP_G);
#define GATE(t) __builtin_amdgcn_rcpf(1.0f + __expf(-(gl[t] + gl[128 + (t)] + gl[256 + (t)] + gl[384 + (t)])))
#pragma unroll
        for (int et = 0; et < 2; ++et) {
            const float om = 1.0f - GATE(64 * th + 32 * et + r32);
#pragma unroll
            for (int e = 0; e < 2; ++e) {
                if (e == 1 && cq == 3) continue;
                const f32x16 a = acc[e][et];
                u32x4 v = {cvtpk(a[0] * a[8] * om, a[1] * a[9] * om), cvtpk(a[2] * a[10] * om, a[3] * a[11] * om),
                           cvtpk(a[4] * a[12] * om, a[5] * a[13] * om), cvtpk(a[6] * a[14] * om, a[7] * a[15] * om)};
                ST16(v, WS_AF + (((2 * tt + th) * KB_ALL + 7 * ct + 2 * cq + e) * 2 + et) * 1024 + lane * 16);
            }
        }
        STAMP(3);
        const int tbl = wid & 3, eh = wid >> 2;
        lds_wait(fl + FL_QDONE, 2);
        const u32x4 qraw = *(const LAS u32x4*)((lds_cptr)lds + SP_Q + eh * 4096 + tbl * 1024 + lane * 16);
        const float gate = GATE(32 * tbl + r32);
        const float qn2 = xhalf_sum(norm2_8(qraw));
        lds_wait(fl + FL_KVFULL, 2);
        STAMP(4);
        const float kmax2 = __uint_as_float(lds_ld(fl + FL_KMAX));
        const float bound = __builtin_amdgcn_sqrtf(qn2 * kmax2) * 1.001f + 1e-6f;
        const bool fast = __all(bound <= 40.0f);
        const bf16x8 qf = __builtin_bit_cast(bf16x8, qraw);
        const lds_cptr kp = (lds_cptr)lds + KVOFF + lane * 16;
        const lds_cptr vp = (lds_cptr)lds + KVOFF + 1024 + (r32 < 16 ? (hi * 16 + r32) * 16 : 512);
        f32x16 o = {};
#define PUBLISH_H() do { VMCNT(0); if (lds_sig_ret(fl + FL_HCNT) == 7u) { const unsigned long long E_ = get_nonce(fl); FLAG_SET(WS_FH + (tt * 16 + ct) * 8, E_); } } while (0)
        if (eh == 0) PUBLISH_H(); else __builtin_amdgcn_s_setprio(1);
        if (fast) {
            f32x16 cneg;
#pragma unroll
            for (int i = 0; i < 16; ++i) cneg[i] = -bound;
            PIN(cneg);
#define EX(S, i) S[i] = __builtin_amdgcn_exp2f(S[i])
#define TILE(SC, SP, OFF, FIRST, QK, KLD, VLD)                                                                     \
            {                                                                                                      \
                EX(SC, 0); EX(SC, 1); PIN(SC); SBAR();                                                             \
                if (!(FIRST)) { o = SMF(v1, hi8(SP), o); SBAR(); v1 = lds_frag(vq + (OFF) + 528); SBAR(); }        \
                EX(SC, 2); EX(SC, 3); EX(SC, 4); EX(SC, 5); EX(SC, 6); PIN(SC); SBAR();                            \
                if (QK) { SP = MFMA32(kn, qf, cneg); SBAR(); }                                                     \
                if (KLD) { kn = lds_frag(kq + (OFF) + 2 * KVREC); SBAR(); }                                        \
                EX(SC, 7); EX(SC, 8); EX(SC, 9); EX(SC, 10); EX(SC, 11); PIN(SC); SBAR();                          \
                o = SMF(v0, lo8(SC), o); SBAR();                                                                   \
                if (VLD) { v0 = lds_frag(vq + (OFF) + KVREC); SBAR(); }                                            \
                EX(SC, 12); EX(SC, 13); EX(SC, 14); EX(SC, 15); PIN(SC); SBAR();                                   \
            }
#define TILE4() do { TILE(sA, sB, 0, false, true, true, true); TILE(sB, sA, KVREC, false, true, true, true);      \
                     TILE(sA, sB, 2 * KVREC, false, true, true, true); TILE(sB, sA, 3 * KVREC, false, true, true, true); \
                     kq += 4 * KVREC; vq += 4 * KVREC; } while (0)
            lds_cptr kq = kp, vq = vp;
            f32x16 sA = MFMA32(lds_frag(kq), qf, cneg), sB;
            bf16x8 kn = lds_frag(kq + KVREC), v0 = lds_frag(vq), v1 = lds_frag(vq + 528);
            TILE(sA, sB, 0, true, true, true, true);
            TILE(sB, sA, KVREC, false, true, true, true);
            kq += 2 * KVREC; vq += 2 * KVREC;
#pragma nounroll
            for (int seg = 0; seg < 5; ++seg) {
                if (seg == 1 && eh == 1) PUBLISH_H();
                if (seg == 2) lds_wait(fl + FL_KVFULL + 1, 2);
                if (seg == 4) lds_wait(fl + FL_KVFULL + 2, 2);
                if (seg == PSWITCH) __builtin_amdgcn_s_setprio(0);
#pragma nounroll
                for (int n = (0x54231 >> (4 * seg)) & 15; n > 0; --n) TILE4();
            }
            TILE(sA, sB, 0, false, true, false, true);
            TILE(sB, sA, KVREC, false, false, false, false);
            o = SMF(v1, hi8(sB), o);
        } else {
            if (eh == 1) PUBLISH_H();
            lds_wait(fl + FL_KVFULL + 1, 2);
            lds_wait(fl + FL_KVFULL + 2, 2);
            float m = -__builtin_inff();
            const f32x16 zero = {};
            for (int t = 0; t < 64; ++t) {
                f32x16 s = MFMA32(lds_frag(kp + t * KVREC), qf, zero);
                const bf16x8 v0 = lds_frag(vp + t * KVREC), v1 = lds_frag(vp + t * KVREC + 528);
                float tm = s[0];
#pragma unroll
                for (int i = 1; i < 16; ++i) tm = __builtin_fmaxf(tm, s[i]);
                tm = xhalf_max(tm);
                const float mn = __builtin_fmaxf(m, tm);
                const float alpha = __builtin_amdgcn_exp2f(m - mn);
                m = mn;
#pragma unroll
                for (int i = 0; i < 16; ++i) { o[i] *= alpha; s[i] = __builtin_amdgcn_exp2f(s[i] - mn); }
                o = SMF(v0, lo8(s), o);
                o = SMF(v1, hi8(s), o);
            }
        }
        STAMP(5);
        const float sc = gate * __builtin_amdgcn_rcpf(o[8]);
        ST16(pack8(o, 0, sc), WS_AF + (((2 * tt + (tbl >> 1)) * KB_ALL + KB_H + 2 * ct + eh) * 2 + (tbl & 1)) * 1024 + lane * 16);
        VMCNT(0);
        if (lds_sig_ret(fl + FL_OCNT + eh) == 3u) { const unsigned long long E_ = get_nonce(fl); FLAG_SET(WS_FO + ((2 * tt + eh) * 16 + ct) * 8, E_); }
        STAMP(6);
        return;
    }
    __builtin_amdgcn_s_setprio(2);
    const int aw = wid - 8;
    const bool kvtask = ((ct + 1) & 3) < 2;
    const int lh = aw & 1;
    const unsigned xvo0 = (lane >> 3) * 1024 + (((lane & 7) ^ (lane >> 4)) << 4), xvo1 = (xvo0 + 8192) ^ 64;
    const unsigned gvo = (lane >> 4) * (2 * F_) + 224 * ct + 32 * min(((lane >> 1) & 7) ^ (2 * (lane >> 4)), 6) + 16 * (lane & 1);
    const unsigned uvo = (lane >> 4) * (2 * F_) + 224 * ct + 32 * min(((lane >> 1) & 7) ^ (2 * (lane >> 4)) ^ 1, 6) + 16 * (lane & 1);
    const unsigned qvo = ((lane >> 4) * 8 + ((lane >> 2) & 3)) * (2 * D_) + 64 * ct + 16 * (lane & 3);
    auto stq = [&](int c, int sub) {
        const int kc = (c + rot) & 7, qd = 2 * lh + sub;
        const unsigned sb = lds0 + ((c + 1) % 3) * STAGE + qd * 4096;
        const char* xb = (const char*)x + (size_t)(tt * 128 + 32 * qd) * 1024 + kc * 128;
        const size_t wo_ = (size_t)(kc * 64 + 16 * qd) * (2 * F_);
#pragma unroll
        for (int i = 0; i < 4; ++i) {
            glds16s(xb + (i >> 1) * 16384, (i & 1) ? xvo1 : xvo0, sb + i * 1024);
            glds16s((const char*)wg + wo_ + i * 8 * F_, gvo, sb + GB + i * 1024);
            glds16s((const char*)wu + wo_ + i * 8 * F_, uvo, sb + UB + i * 1024);
        }
        glds16s((const char*)wq + (size_t)(kc * 64 + 32 * lh + 4 * sub) * (2 * D_), qvo, sb + QB - qd * 3072);
    };
    if (aw < 2) {
        glds16s(gw, lane * 16, lds0 + SP_GW);
        int pend = 0;
#pragma nounroll
        for (int j = 0; j < 14; ++j) {
            const int c = j < 4 ? j >> 1 : (j >> 1) + 1, sub = j & 1;
            if (j == 1 && !kvtask) continue;
            if (j == 2) asm volatile("s_barrier" ::: "memory");
            if (j >= 4 && sub == 0) {
                if ((unsigned)__builtin_amdgcn_readfirstlane(lds_ld(fl + FL_DONE + c - 3)) < 8u) {
                    VMCNT(0); lds_sig2(fl + FL_FULL + pend); pend = -1;
                    lds_wait(fl + FL_DONE + c - 3, 8);
                }
                CBAR();
            }
            stq(c, sub);
            if (j >= 2 && sub == 0 && pend >= 0) {
                VMCNT(13);
                if (j == 2 && !kvtask) lds_sig(fl + FL_FULL); else lds_sig2(fl + FL_FULL + pend);
            }
            if (sub == 1) pend = c;
        }
        VMCNT(0); lds_sig2(fl + FL_FULL + 7);
        STAMP(1);
        if (aw == 0) {
            const int rp0 = (b & 31) * 9, rpb = (rp0 + 1) >> 1, rpe = (rp0 + 10) >> 1;
            lds_wait(fl + FL_DONE + 7, 8);
#pragma unroll
            for (int i = 0; i < 5; ++i) {
                const int kb = min(rpb + i, rpe - 1);
                const char* src = (const char*)(kb < KB_H ? wd + (size_t)kb * 16 * D_ : wo + (size_t)(kb - KB_H) * 16 * D_) + 128 * (b >> 5);
                glds16s(src, (lane >> 3) * 1024 + (lane & 7) * 16, lds0 + SP_RT + i * 2048);
                glds16s(src + 8192, (lane >> 3) * 1024 + (lane & 7) * 16, lds0 + SP_RT + i * 2048 + 1024);
            }
            VMCNT(0);
            const lds_cptr tb0 = (lds_cptr)lds + SP_RT + (4 * hi + ((lane & 15) >> 2)) * 128 + ((lane >> 4) & 1) * 32 + (lane & 3) * 8;
#pragma unroll
            for (int i = 0; i < 5; ++i) {
                const int kb = min(rpb + i, rpe - 1);
#pragma unroll
                for (int en = 0; en < 2; ++en)
                    ST16(__builtin_bit_cast(u32x4, tr_frag(tb0 + i * 2048 + en * 64, tb0 + i * 2048 + en * 64 + 1024)), WS_WF + (((b >> 5) * KB_ALL + kb) * 2 + en) * 1024 + lane * 16);
            }
            VMCNT(0);
            const unsigned long long E_ = get_nonce(fl);
            FLAG_SET(WS_FW + b * 8, E_);
        }
        STAMP(2);
    } else {
        const int kh = aw - 2;
        const int tb = 4 * tt + (ct >> 2), isv = ct & 1;
        const unsigned long long chal = __builtin_amdgcn_s_memrealtime();
        if (aw == 3) FLAG_SET(WS_CHAL + b * 8, chal);
        if (!kvtask) { stq(0, 1); asm volatile("s_barrier" ::: "memory"); }
        if (b == LEADER && aw == 3) {
            if (lane == 0) { __hip_atomic_store(fl + FL_E, (unsigned)chal, RLX_WG); __hip_atomic_store(fl + FL_E + 1, (unsigned)(chal >> 32), RLX_WG); }
            lds_sig(fl + FL_GO);
        }
        const int q4 = (lane & 15) >> 2, p4 = lane & 3;
        f32x16 acc = {};
        if (kvtask) {
            const char* xb = (const char*)x + (size_t)(32 * tb) * 1024 + 512 * kh;
#pragma unroll
            for (int i = 0; i < 16; ++i) {
                glds16s(xb + i * 2048, hi * 1024 + ((r32 ^ ((2 * i + hi) & 15)) << 4), lds0 + kh * 16384 + i * 1024);
            }
            const char* wb = (const char*)(isv ? wv : wk) + 8192 * kh;
#pragma unroll
            for (int i = 0; i < 8; ++i) glds16s(wb + i * 1024, lane * 16, lds0 + 32768 + kh * 8192 + i * 1024);
            asm volatile("s_barrier" ::: "memory");
            VMCNT(0);
            const lds_cptr xl = (lds_cptr)lds + kh * 16384 + r32 * 512, wl = (lds_cptr)lds + 32768 + kh * 8192 + (8 * hi + q4) * 32 + p4 * 8;
#pragma unroll
            for (int ks = 0; ks < 16; ++ks) {
                const bf16x8 xk = lds_frag(xl + (((2 * ks + hi) ^ (r32 & 15)) << 4)), wf = tr_frag(wl + ks * 512, wl + ks * 512 + 128);
                const bf16x8 ma = isv ? xk : wf, mb = isv ? wf : xk;
                acc = MFMA32(ma, mb, acc);
            }
            LAS f32x4* red = (LAS f32x4*)((lds_ptr)lds + SP_RED) + lane;
            if (kh == 1) {
#pragma unroll
                for (int g = 0; g < 4; ++g) red[g * 64] = f32x4{acc[4 * g], acc[4 * g + 1], acc[4 * g + 2], acc[4 * g + 3]};
                lds_sig(fl + FL_KVRED);
                lds_wait(fl + FL_KVSLOT, 1);
            } else {
                lds_wait(fl + FL_KVRED, 1);
#pragma unroll
                for (int g = 0; g < 4; ++g) { const f32x4 p = red[g * 64]; acc[4 * g] += p[0]; acc[4 * g + 1] += p[1]; acc[4 * g + 2] += p[2]; acc[4 * g + 3] += p[3]; }
                PIN(acc);
                lds_sig(fl + FL_KVSLOT);
            }
        }
#pragma nounroll
        for (int sub = 0; sub < 2; ++sub) {
            stq(2, sub);
            if (!kvtask && sub == 0) { VMCNT(13); lds_sig(fl + FL_FULL); }
        }
        VMCNT(0);
        lds_sig2(fl + FL_FULL + 2);
        STAMP(1);
        unsigned long long E;
        if (aw == 3) {
            E = chal;
            if (b != LEADER) {
#pragma nounroll
                for (unsigned s = 0; s < SPIN_CAP; ++s) {
                    CBAR();
                    const u32x4 r = __builtin_amdgcn_raw_buffer_load_b128(rs, WS_RESP + b * 16, 0, 16);
                    E = (unsigned long long)r[2] | ((unsigned long long)r[3] << 32);
                    if (__all(((unsigned long long)r[0] | ((unsigned long long)r[1] << 32)) == (chal ^ E))) break;
                    __builtin_amdgcn_s_sleep(24);
                }
            }
            if (b != LEADER) {
                if (lane == 0) { __hip_atomic_store(fl + FL_E, (unsigned)E, RLX_WG); __hip_atomic_store(fl + FL_E + 1, (unsigned)(E >> 32), RLX_WG); }
                lds_sig(fl + FL_GO);
            }
            FLAG_SET(WS_ACK + b * 8, E);
            STAMP(12);
            if (b == LEADER) {
#pragma nounroll
                for (unsigned s = 0; s < SPIN_CAP; ++s) {
                    if (echo_round(rs, lane, E)) break;
                    __builtin_amdgcn_s_sleep(32);
                }
            }
            STAMP(2);
            lds_wait(fl + FL_KVOK, 1);
        } else {
            if (kvtask) {
                const int rec = WS_KV + tb * KVREC;
                if (isv) {
                    if (r32 < 16) {
#pragma unroll
                        for (int g = 0; g < 4; ++g) {
                            const u32x2 v = {cvtpk(acc[4 * g], acc[4 * g + 1]), cvtpk(acc[4 * g + 2], acc[4 * g + 3])};
                            ST8(v, rec + 1024 + (g >> 1) * 528 + ((g & 1) * 16 + r32) * 16 + hi * 8);
                        }
                    } else if (r32 == 16) {
                        const u32x4 ones = {0x3f803f80u, 0x3f803f80u, 0x3f803f80u, 0x3f803f80u};
                        ST16(ones, rec + 1024 + 512 + hi * 528);
                    }
                } else {
                    const u32x4 kv = pack8n(acc, 0);
                    ST16(kv, rec + lane * 16);
                    float n2 = xhalf_sum(norm2_8(kv));
#pragma unroll
                    for (int of = 1; of < 32; of <<= 1) n2 = __builtin_fmaxf(n2, __shfl_xor(n2, of));
                    if (lane == 0) __hip_atomic_store((gu32*)(ws + WS_KMAX) + tb, __float_as_uint(n2), RLX_AGENT);
                }
                VMCNT(0);
            }
            E = get_nonce(fl);
            if (kvtask) FLAG_SET(WS_FKV + (2 * tb + isv) * 8, E);
            STAMP(2);
            lds_wait(fl + FL_DONE + 4, 8);
#pragma nounroll
            for (unsigned s = 0; s < SPIN_CAP; ++s) {
                CBAR();
                const u32x4 f = __builtin_amdgcn_raw_buffer_load_b128(rs, WS_FKV + lane * 16, 0, 16);
                if (__all(f[0] == (unsigned)E && f[2] == (unsigned)E && f[1] == (unsigned)(E >> 32) && f[3] == (unsigned)(E >> 32))) break;
                __builtin_amdgcn_s_sleep(4);
            }
            CBAR();
            lds_sig(fl + FL_KVOK);
            asm volatile("s_mov_b32 m0, %2\n\ts_nop 0\n\tglobal_load_lds_dword %0, %1 sc1" :: "v"(lane * 4), "s"(ws + WS_KMAX), "s"(lds0 + SP_KMAX) : "memory");
        }
        STAMP(3);
#pragma nounroll
        for (int p = 0; p < 3; ++p) {
            lds_wait(fl + FL_DONE + 5 + p, 8);
            const int n = p == 0 ? 22 : p == 1 ? 26 : 17, u0 = (p == 0 ? 0 : p == 1 ? 44 : 96) + n * kh;
            int i = 0;
#pragma nounroll
            for (; i + 4 <= n; i += 4) glds16x4_sc1(ws + WS_KV + (u0 + i) * 1024, lane * 16, lds0 + KVOFF + (u0 + i) * 1024);
#pragma nounroll
            for (; i < n; ++i) glds16x1_sc1(ws + WS_KV + (u0 + i) * 1024, lane * 16, lds0 + KVOFF + (u0 + i) * 1024);
            if (p == 0 && aw == 2) {
                VMCNT(22);
                float km = ((const LAS float*)((lds_cptr)lds + SP_KMAX))[lane];
#pragma unroll
                for (int of = 1; of < 64; of <<= 1) km = __builtin_fmaxf(km, __shfl_xor(km, of));
                if (lane == 0) __hip_atomic_store(fl + FL_KMAX, __float_as_uint(km), RLX_WG);
            }
            if (p == 1) { VMCNT(26); lds_sig(fl + FL_KVFULL); }
            if (p == 2) { VMCNT(17); lds_sig(fl + FL_KVFULL + 1); VMCNT(0); lds_sig(fl + FL_KVFULL + 2); }
        }
        STAMP(4);
    }
    const int tp = 4 * xcd + (idx >> 3), np = idx & 7;
    const int abase = WS_AF + tp * KB_ALL * 2048 + lane * 16, wbase = WS_WF + np * KB_ALL * 2048 + lane * 16;
    const int rot2 = (7 * (np & 3) + 2 * (idx >> 3)) % 28;
    f32x16 acc[2][2] = {};
    constexpr int WIN = 5;
    bf16x8 fa[WIN][2], fw[WIN][2];
    auto kbof = [&](int s) { int k = s + rot2; k = k >= 28 ? k - 28 : k; return (28 * aw + k) * 2048; };
    __builtin_amdgcn_s_setprio(0);
    const unsigned long long E = get_nonce(fl);
    glb_wait<8>((gu64*)(ws + WS_FW) + np * 32 + r32, E);
#pragma unroll
    for (int s = 0; s < WIN; ++s) { const int o = kbof(s); fw[s][0] = LD16(wbase + o); fw[s][1] = LD16(wbase + o + 1024); }
    glb_wait<8>((gu64*)(ws + WS_FH) + (tp >> 1) * 16 + (lane & 15), E);
    STAMP(5);
#pragma unroll
    for (int s = 0; s < WIN; ++s) { const int o = kbof(s); fa[s][0] = LD16(abase + o); fa[s][1] = LD16(abase + o + 1024); }
    SBAR();
#define MM4(S) do { acc[0][0] = MFMA32(fw[S][0], fa[S][0], acc[0][0]); acc[0][1] = MFMA32(fw[S][0], fa[S][1], acc[0][1]); \
                    acc[1][0] = MFMA32(fw[S][1], fa[S][0], acc[1][0]); acc[1][1] = MFMA32(fw[S][1], fa[S][1], acc[1][1]); } while (0)
#pragma unroll
    for (int s = 0; s < 28; ++s) {
        MM4(s % WIN);
        SBAR();
        if (s + WIN < 28) {
            const int o = kbof(s + WIN);
            fw[s % WIN][0] = LD16(wbase + o); fw[s % WIN][1] = LD16(wbase + o + 1024);
            fa[s % WIN][0] = LD16(abase + o); fa[s % WIN][1] = LD16(abase + o + 1024);
            SBAR();
        }
    }
    STAMP(6);
#pragma unroll
    for (int e = 0; e < 2; ++e) {
#pragma unroll
        for (int s = 0; s < 4; ++s) { const int o = (KB_H + 2 * (4 * aw + s) + e) * 2048; fw[s][0] = LD16(wbase + o); fw[s][1] = LD16(wbase + o + 1024); }
        glb_wait<2>((gu64*)(ws + WS_FO) + (2 * (tp >> 1) + e) * 16 + (lane & 15), E);
        STAMP(7 + 2 * e);
#pragma unroll
        for (int s = 0; s < 4; ++s) { const int o = (KB_H + 2 * (4 * aw + s) + e) * 2048; fa[s][0] = LD16(abase + o); fa[s][1] = LD16(abase + o + 1024); }
        SBAR();
#pragma unroll
        for (int s = 0; s < 4; ++s) MM4(s);
        SBAR();
        STAMP(8 + 2 * e);
    }
    asm volatile("s_barrier" ::: "memory");
    {
        lds_ptr mine = (lds_ptr)lds + aw * RED_TILE;
#pragma unroll
        for (int en = 0; en < 2; ++en)
#pragma unroll
            for (int et = 0; et < 2; ++et)
#pragma unroll
                for (int g = 0; g < 4; ++g)
                    *(LAS f32x4*)(mine + (32 * et + r32) * RED_ROW + (32 * en + 8 * g + 4 * hi) * 4) =
                        f32x4{acc[en][et][4 * g], acc[en][et][4 * g + 1], acc[en][et][4 * g + 2], acc[en][et][4 * g + 3]};
    }
    asm volatile("s_waitcnt lgkmcnt(0)\n\ts_barrier" ::: "memory");
#pragma unroll
    for (int h = 0; h < 2; ++h) {
        const int tok = 16 * aw + 8 * h + (lane >> 3), oc = lane & 7;
        f32x4 s0 = {}, s1 = {};
#pragma unroll
        for (int w = 0; w < 4; ++w) {
            const lds_cptr p = (lds_cptr)lds + w * RED_TILE + tok * RED_ROW + oc * 32;
            s0 += *(const LAS f32x4*)p; s1 += *(const LAS f32x4*)(p + 16);
        }
        const u32x4 v = {cvtpk(s0[0], s0[1]), cvtpk(s0[2], s0[3]), cvtpk(s1[0], s1[1]), cvtpk(s1[2], s1[3])};
        *(u32x4*)(out + (size_t)(64 * tp + tok) * D_ + 64 * np + 8 * oc) = v;
    }
    STAMP(11);
}

extern "C" void kernel_launch(void* const* d_in, const int* in_sizes, int n_in, void* d_out, int out_size, void* d_ws,
                              size_t ws_size, hipStream_t stream) {
    fused<<<256, 768, 0, stream>>>((const u16*)d_in[0], (const u16*)d_in[1], (const u16*)d_in[2], (const u16*)d_in[3], (const u16*)d_in[4],
                                   (const u16*)d_in[6], (const u16*)d_in[7], (const u16*)d_in[8], (const u16*)d_in[9], (char*)d_ws, (u16*)d_out);
}
```

```cpp
#include <hip/hip_runtime.h>
#include <hip/hip_bf16.h>
#include <stdint.h>

typedef __attribute__((ext_vector_type(2))) __bf16 bf16x2;
typedef __attribute__((ext_vector_type(8))) __bf16 bf16x8;
typedef __attribute__((ext_vector_type(16))) __bf16 bf16x16;
typedef __attribute__((ext_vector_type(16))) float f32x16;
typedef __attribute__((ext_vector_type(8))) float f32x8;
typedef __attribute__((ext_vector_type(4))) float f32x4;
typedef __attribute__((ext_vector_type(4))) unsigned u32x4;
typedef __attribute__((ext_vector_type(2))) unsigned u32x2;
typedef short v4i16_t __attribute__((ext_vector_type(4)));
typedef short v8i16_t __attribute__((ext_vector_type(8)));
typedef unsigned short u16;
#define LAS __attribute__((address_space(3)))
#define GAS __attribute__((address_space(1)))
typedef LAS const char* lds_cptr;
typedef LAS char* lds_ptr;
typedef LAS unsigned lu32;
typedef GAS unsigned gu32;
typedef GAS unsigned long long gu64;

constexpr int D_ = 512, F_ = 1792, NH = 32;
constexpr int KB_H = F_ / 16;
constexpr int KB_ALL = KB_H + NH;
constexpr float QSCALE = 0.25f * 1.4426950408889634f;

constexpr int WS_CHAL = 0, WS_ACK = 2048, WS_RESP = 4096;
constexpr int WS_FKV = 8192, WS_FH = 9216, WS_FW = 11264, WS_FO = 13312;
constexpr int WS_KMAX = 32768;
constexpr int KVREC = 2080, KVIMG = 64 * KVREC;
constexpr int WS_KV = 65536;
constexpr int WS_AF = 262144;
constexpr int WS_WF = WS_AF + 32 * KB_ALL * 2048;
constexpr int WS_END = WS_WF + 8 * KB_ALL * 2048;
constexpr int LEADER = 64;

constexpr int GB = 16384, UB = 32768, QB = 49152, STAGE = 53248, RING = 3 * STAGE;
constexpr int SP_FLAG = RING, SP_KMAX = RING + 256, SP_DUMP = RING + 512, SP_GW = RING + 1024, SP_G = RING + 2048, LDS_BYTES = RING + 4096;
constexpr int SP_Q = 0, KVOFF = 8192, SP_RT = KVOFF + KVIMG, SP_RED = 49152, SP_H = RING - 28672;
enum { FL_FULL = 32, FL_DONE = 8, FL_KVFULL = 16, FL_HREAD = 0, FL_HST = 1, FL_GO = 20, FL_KVOK = 21, FL_GDONE = 22, FL_QDONE = 23, FL_HCNT = 24, FL_OCNT = 25, FL_KVRED = 27, FL_KMAX = 28, FL_KVSLOT = 29, FL_E = 30 };
constexpr int RED_ROW = 272, RED_TILE = 64 * RED_ROW;

#define STAMP_DECL()
#define STAMP(i)
#define STAMP_FLUSH()
#define AHEAD 1
#define PREFETCH 0
#define PSWITCH 2
#define ECHO0 12
#define ECHO1 16
#define BPRIO 2
#define BSPREAD 1
#define APRIO 5
#define ABL 0
#define MFMA32(a, b, c) __builtin_amdgcn_mfma_f32_32x32x16_bf16(a, b, c, 0, 0, 0)
#define SMF(a, p8, c) __builtin_amdgcn_smfmac_f32_32x32x32_bf16(a, __builtin_bit_cast(bf16x16, p8), c, 0xDDDDDDDD, 0, 0)
#define SBAR() __builtin_amdgcn_sched_barrier(0)
#define PIN(x) asm volatile("" : "+v"(x))
#define CBAR() asm volatile("" ::: "memory")
#define RLX_AGENT __ATOMIC_RELAXED, __HIP_MEMORY_SCOPE_AGENT
#define RLX_WG __ATOMIC_RELAXED, __HIP_MEMORY_SCOPE_WORKGROUP
#define VMCNT(n) asm volatile("s_waitcnt vmcnt(" #n ")" ::: "memory")
constexpr unsigned SPIN_CAP = 1u << 16;

__device__ __forceinline__ void glds16(const void* g, unsigned lds_base) {
    lds_base = __builtin_amdgcn_readfirstlane(lds_base);
    asm volatile("s_mov_b32 m0, %1\n\ts_nop 0\n\tglobal_load_lds_dwordx4 %0, off" :: "v"(g), "s"(lds_base) : "memory");
}
__device__ __forceinline__ void glds16s(const void* sbase, unsigned voff, unsigned lds_base) {
    asm volatile("s_mov_b32 m0, %2\n\ts_nop 0\n\tglobal_load_lds_dwordx4 %0, %1" :: "v"(voff), "s"(sbase), "s"(lds_base) : "memory");
}
__device__ __forceinline__ void glds16x4_sc1(const void* sbase, unsigned voff, unsigned lds_base) {
    asm volatile("s_mov_b32 m0, %2\n\ts_nop 0\n\tglobal_load_lds_dwordx4 %0, %1 sc1\n\tglobal_load_lds_dwordx4 %0, %1 offset:1024 sc1\n\t"
                 "global_load_lds_dwordx4 %0, %1 offset:2048 sc1\n\tglobal_load_lds_dwordx4 %0, %1 offset:3072 sc1" :: "v"(voff), "s"(sbase), "s"(lds_base) : "memory");
}
__device__ __forceinline__ void glds16x1_sc1(const void* sbase, unsigned voff, unsigned lds_base) {
    asm volatile("s_mov_b32 m0, %2\n\ts_nop 0\n\tglobal_load_lds_dwordx4 %0, %1 sc1" :: "v"(voff), "s"(sbase), "s"(lds_base) : "memory");
}
__device__ __forceinline__ unsigned cvtpk(float lo, float hi) {
    typedef __attribute__((ext_vector_type(2))) float f32x2;
    f32x2 v = {lo, hi};
    return __builtin_bit_cast(unsigned, __builtin_convertvector(v, bf16x2));
}
__device__ __forceinline__ u32x4 pack8(const f32x16& a, int o, float s) {
    return u32x4{cvtpk(a[o] * s, a[o + 1] * s), cvtpk(a[o + 2] * s, a[o + 3] * s), cvtpk(a[o + 4] * s, a[o + 5] * s), cvtpk(a[o + 6] * s, a[o + 7] * s)};
}
__device__ __forceinline__ u32x4 pack8n(const f32x16& a, int o) {
    return u32x4{cvtpk(a[o], a[o + 1]), cvtpk(a[o + 2], a[o + 3]), cvtpk(a[o + 4], a[o + 5]), cvtpk(a[o + 6], a[o + 7])};
}
__device__ __forceinline__ float bf_lo(unsigned u) { return __uint_as_float(u << 16); }
__device__ __forceinline__ float bf_hi(unsigned u) { return __uint_as_float(u & 0xffff0000u); }
__device__ __forceinline__ bf16x8 lds_frag(lds_cptr p) { return *(const LAS bf16x8*)p; }
__device__ __forceinline__ v4i16_t lds_tr(lds_cptr p) { return __builtin_amdgcn_ds_read_tr16_b64_v4i16((LAS v4i16_t*)p); }
__device__ __forceinline__ bf16x8 tr_frag(lds_cptr p0, lds_cptr p1) {
    v4i16_t a = lds_tr(p0), b = lds_tr(p1);
    v8i16_t r = {a[0], a[1], a[2], a[3], b[0], b[1], b[2], b[3]};
    return __builtin_bit_cast(bf16x8, r);
}
__device__ __forceinline__ float xhalf_sum(float v) {
    auto rr = __builtin_amdgcn_permlane32_swap(__float_as_uint(v), __float_as_uint(v), false, false);
    return __uint_as_float(rr[0]) + __uint_as_float(rr[1]);
}
__device__ __forceinline__ float xhalf_max(float v) {
    auto rr = __builtin_amdgcn_permlane32_swap(__float_as_uint(v), __float_as_uint(v), false, false);
    return __builtin_fmaxf(__uint_as_float(rr[0]), __uint_as_float(rr[1]));
}
__device__ __forceinline__ f32x8 lo8(const f32x16& v) { return __builtin_shufflevector(v, v, 0, 1, 2, 3, 4, 5, 6, 7); }
__device__ __forceinline__ f32x8 hi8(const f32x16& v) { return __builtin_shufflevector(v, v, 8, 9, 10, 11, 12, 13, 14, 15); }
__device__ __forceinline__ float norm2_8(const u32x4& v) {
    float n = 0.f;
#pragma unroll
    for (int i = 0; i < 4; ++i) { const float a = bf_lo(v[i]), b = bf_hi(v[i]); n += a * a + b * b; }
    return n;
}
__device__ __forceinline__ unsigned lds_ld(lu32* p) { return __hip_atomic_load(p, RLX_WG); }
__device__ __forceinline__ void lds_sig(lu32* p) {
    asm volatile("s_mov_b64 exec, 1\n\tds_add_u32 %0, %1\n\ts_mov_b64 exec, -1" :: "v"(p), "v"(1u) : "memory");
}
__device__ __forceinline__ void lds_sig2(lu32* p) {
    asm volatile("s_mov_b64 exec, 1\n\tds_add_u32 %0, %1\n\ts_mov_b64 exec, -1" :: "v"(p), "v"(2u) : "memory");
}
__device__ __forceinline__ unsigned lds_sig_ret(lu32* p) {
    unsigned r;
    asm volatile("s_mov_b64 exec, 1\n\tds_add_rtn_u32 %0, %1, %2\n\ts_waitcnt lgkmcnt(0)\n\ts_mov_b64 exec, -1" : "=&v"(r) : "v"(p), "v"(1u) : "memory");
    return __builtin_amdgcn_readfirstlane(r);
}
__device__ __forceinline__ void lds_wait(lu32* p, unsigned want) {
#pragma nounroll
    for (unsigned s = 0; s < SPIN_CAP; ++s) {
        CBAR();
        if ((unsigned)__builtin_amdgcn_readfirstlane(lds_ld(p)) >= want) break;
        __builtin_amdgcn_s_sleep(1);
    }
    CBAR();
}
template <int SLEEP>
__device__ __forceinline__ void glb_wait(gu64* p, unsigned long long E) {
#pragma nounroll
    for (unsigned s = 0; s < SPIN_CAP; ++s) {
        CBAR();
        if (__all(__hip_atomic_load(p, RLX_AGENT) == E)) break;
        __builtin_amdgcn_s_sleep(SLEEP);
    }
    CBAR();
}
__device__ __forceinline__ unsigned long long get_nonce(lu32* fl) {
    lds_wait(fl + FL_GO, 1);
    return (unsigned long long)lds_ld(fl + FL_E) | ((unsigned long long)lds_ld(fl + FL_E + 1) << 32);
}
#define FLAG_SET(off, E) do { if (lane == 0) __hip_atomic_store((gu64*)(ws + (off)), E, RLX_AGENT); } while (0)
#define ST16(v, off) __builtin_amdgcn_raw_buffer_store_b128(v, rs, off, 0, 16)
#define ST8(v, off) __builtin_amdgcn_raw_buffer_store_b64(v, rs, off, 0, 16)
#define LD16(off) __builtin_bit_cast(bf16x8, __builtin_amdgcn_raw_buffer_load_b128(rs, off, 0, 16))

__device__ __forceinline__ bool echo_round(const __amdgpu_buffer_rsrc_t rs, int lane, unsigned long long E) {
    const unsigned e0 = (unsigned)E, e1 = (unsigned)(E >> 32);
    CBAR();
    const u32x4 c0 = __builtin_amdgcn_raw_buffer_load_b128(rs, WS_CHAL + lane * 32, 0, 16), c1 = __builtin_amdgcn_raw_buffer_load_b128(rs, WS_CHAL + lane * 32 + 16, 0, 16);
    const u32x4 a0 = __builtin_amdgcn_raw_buffer_load_b128(rs, WS_ACK + lane * 32, 0, 16), a1 = __builtin_amdgcn_raw_buffer_load_b128(rs, WS_ACK + lane * 32 + 16, 0, 16);
    if (__all(a0[0] == e0 && a0[1] == e1 && a0[2] == e0 && a0[3] == e1 && a1[0] == e0 && a1[1] == e1 && a1[2] == e0 && a1[3] == e1)) return true;
    ST16((u32x4{c0[0] ^ e0, c0[1] ^ e1, e0, e1}), WS_RESP + lane * 64);
    ST16((u32x4{c0[2] ^ e0, c0[3] ^ e1, e0, e1}), WS_RESP + lane * 64 + 16);
    ST16((u32x4{c1[0] ^ e0, c1[1] ^ e1, e0, e1}), WS_RESP + lane * 64 + 32);
    ST16((u32x4{c1[2] ^ e0, c1[3] ^ e1, e0, e1}), WS_RESP + lane * 64 + 48);
    return false;
}

__global__ void __launch_bounds__(768) fused(const u16* __restrict__ x, const u16* __restrict__ wq, const u16* __restrict__ wk,
                                             const u16* __restrict__ wv, const u16* __restrict__ wo, const u16* __restrict__ wg,
                                             const u16* __restrict__ wu, const u16* __restrict__ wd, const u16* __restrict__ gw,
                                             char* ws, u16* __restrict__ out) {
    __shared__ __attribute__((aligned(16))) char lds[LDS_BYTES];
    const int b = blockIdx.x, xcd = b & 7, idx = b >> 3;
    const int lane = threadIdx.x & 63, wid = __builtin_amdgcn_readfirstlane(threadIdx.x >> 6);
    const int r32 = lane & 31, hi = lane >> 5;
    const int tt = 8 * (xcd & 1) + (idx & 7), ct = 4 * (xcd >> 1) + (idx >> 3);
#define ROTMODE 0
    const int rot = ROTMODE == 0 ? 0 : ROTMODE == 1 ? 4 * ((idx ^ (idx >> 3)) & 1) : ((idx & 7) + 2 * (idx >> 3)) & 7;
    const unsigned lds0 = (unsigned)(uintptr_t)lds;
    lu32* const fl = (lu32*)((lds_ptr)lds + SP_FLAG);
    const __amdgpu_buffer_rsrc_t rs = __builtin_amdgcn_make_buffer_rsrc(ws, 0, WS_END, 0x00020000);
    STAMP_DECL();
    STAMP(0);
    if (wid == 0) fl[lane] = lane == FL_FULL + 8 ? 4u : 0u;
    if (wid < 8) {
        asm volatile("s_waitcnt lgkmcnt(0)\n\ts_barrier" ::: "memory");
        if (b == LEADER && wid >= 6) {
            const unsigned long long E = get_nonce(fl);
            __builtin_amdgcn_s_sleep(ECHO0);
            if (wid == 7) __builtin_amdgcn_s_sleep(ECHO1);
            echo_round(rs, lane, E);
        }
        if (PREFETCH && wid < 5) {
            const int id = idx * 64 + lane, kc = (3 + wid + rot) & 7, kr = kc * 64 + ((id >> 4) & 63), l = id & 15;
            const char* pa = id < 1024 ? (const char*)x + (size_t)(1024 * (xcd & 1) + id) * 1024 + kc * 128
                           : l < 14 ? (const char*)(l < 7 ? wg : wu) + (size_t)kr * (2 * F_) + 896 * (xcd >> 1) + 128 * (l < 7 ? l : l - 7)
                                    : (const char*)wq + (size_t)kr * (2 * D_) + 256 * (xcd >> 1) + 128 * (l - 14);
            asm volatile("s_mov_b32 m0, %1\n\ts_nop 0\n\tglobal_load_lds_dword %0, off\n\tglobal_load_lds_dword %0, off offset:64" :: "v"(pa), "s"(lds0 + SP_DUMP) : "memory");
        }
        if (PREFETCH && wid == 5) {
            const int rb = (((b & 31) * 9 + 1) >> 1) * 16;
#pragma unroll
            for (int i = 0; i < 2; ++i) {
                const int r = min(rb + 64 * i + lane, F_ + D_ - 1);
                const char* pa = (const char*)(r < F_ ? wd + (size_t)r * D_ : wo + (size_t)(r - F_) * D_) + 128 * (b >> 5);
                if (i == 0 || lane < 16)
                    asm volatile("s_mov_b32 m0, %1\n\ts_nop 0\n\tglobal_load_lds_dword %0, off\n\tglobal_load_lds_dword %0, off offset:64" :: "v"(pa), "s"(__builtin_amdgcn_readfirstlane(lds0 + SP_DUMP)) : "memory");
            }
        }
        const int th = wid & 1, cq = wid >> 1;
        int xoff[4], woff[2][4];
        {
            const int row = 64 * th + r32, q4 = (lane & 15) >> 2, p4 = lane & 3, sel = (lane >> 4) & 1;
#pragma unroll
            for (int j = 0; j < 4; ++j) {
                xoff[j] = row * 128 + (((2 * (cq ^ j) + hi) ^ ((row >> 1) & 7)) << 4);
#pragma unroll
                for (int e = 0; e < 2; ++e) {
                    const int ks = cq ^ j, L = 2 * cq + e;
                    woff[e][j] = L < 7 ? GB + sel * 16384 + (16 * ks + 8 * hi + q4) * 256 + ((L ^ (2 * q4) ^ sel) << 5) + p4 * 8
                                       : QB + (ks >> 1) * 2048 + (2 * (ks & 1) + hi) * 256 + q4 * 64 + sel * 32 + p4 * 8;
                }
            }
        }
        f32x16 acc[2][2] = {};
        bf16x8 xf[4][2]; v4i16_t wa[4][2], wb[4][2];
        unsigned ax, a0, a1;
#define ADDR(J) do { ax = so + xoff[J]; a0 = so + woff[0][J]; a1 = so + woff[1][J]; PIN(ax); PIN(a0); PIN(a1); } while (0)
#define LP(a) ((lds_cptr)(uintptr_t)(a))
#define WF(B, E) __builtin_bit_cast(bf16x8, (v8i16_t{wa[B][E][0], wa[B][E][1], wa[B][E][2], wa[B][E][3], wb[B][E][0], wb[B][E][1], wb[B][E][2], wb[B][E][3]}))
#define LOADALL(N) do { xf[N][0] = lds_frag(LP(ax)); wa[N][0] = lds_tr(LP(a0)); wb[N][0] = lds_tr(LP(a0) + 1024); xf[N][1] = lds_frag(LP(ax) + 4096); \
            wa[N][1] = lds_tr(LP(a1)); wb[N][1] = lds_tr(LP(a1) + 1024); } while (0)
#define STEP(B, N) do { SBAR();                                                                                                      \
            acc[0][0] = MFMA32(WF(B, 0), xf[B][0], acc[0][0]); SBAR(); xf[N][0] = lds_frag(LP(ax)); wa[N][0] = lds_tr(LP(a0)); SBAR();              \
            acc[0][1] = MFMA32(WF(B, 0), xf[B][1], acc[0][1]); SBAR(); wb[N][0] = lds_tr(LP(a0) + 1024); xf[N][1] = lds_frag(LP(ax) + 4096); SBAR(); \
            acc[1][0] = MFMA32(WF(B, 1), xf[B][0], acc[1][0]); SBAR(); wa[N][1] = lds_tr(LP(a1)); wb[N][1] = lds_tr(LP(a1) + 1024); SBAR();         \
            acc[1][1] = MFMA32(WF(B, 1), xf[B][1], acc[1][1]); SBAR(); } while (0)
        float ga0 = 0.f, ga1 = 0.f;
        const lds_cptr gwp = (lds_cptr)lds + SP_GW + cq * 32 + hi * 16;
        unsigned so = lds0 + STAGE;
        ADDR(0);
        lds_wait(fl + FL_FULL, 4);
        STAMP(1);
        u32x4 gv = *(const LAS u32x4*)(gwp + (rot & 7) * 128);
        if (wid >= 4) __builtin_amdgcn_s_setprio(1);
        LOADALL(0); ADDR(1); LOADALL(1); ADDR(2);
#pragma nounroll
        for (int c = 0; c < 8; ++c) {
            if (c == APRIO) __builtin_amdgcn_s_setprio(0);
            const unsigned nf = lds_ld(fl + FL_FULL + c + 1);
            STEP(0, 2);
            ADDR(3);
            {
                const u32x4 x0 = __builtin_bit_cast(u32x4, xf[0][0]), x1 = __builtin_bit_cast(u32x4, xf[0][1]);
#pragma unroll
                for (int j = 0; j < 4; ++j) {
                    const unsigned g_ = gv[j], a_ = x0[j], b_ = x1[j];
                    ga0 = __builtin_amdgcn_fdot2_f32_bf16(__builtin_bit_cast(bf16x2, a_), __builtin_bit_cast(bf16x2, g_), ga0, false);
                    ga1 = __builtin_amdgcn_fdot2_f32_bf16(__builtin_bit_cast(bf16x2, b_), __builtin_bit_cast(bf16x2, g_), ga1, false);
                }
                PIN(ga0); PIN(ga1);
            }
            STEP(1, 3);
            lds_sig(fl + FL_DONE + c);
            so = so == lds0 + 2 * STAGE ? lds0 : so + STAGE;
            ADDR(0);
            if (c == 7) {
                const float s0 = xhalf_sum(ga0), s1 = xhalf_sum(ga1);
                ((LAS float*)((lds_ptr)lds + SP_G))[cq * 128 + 64 * th + lane] = hi ? s1 : s0;
                lds_sig(fl + FL_GDONE);
            }
            if ((unsigned)__builtin_amdgcn_readfirstlane(nf) < 4u) lds_wait(fl + FL_FULL + c + 1, 4);
            CBAR();
            STEP(2, 0);
            ADDR(1);
            gv = *(const LAS u32x4*)(gwp + ((c + 1 + rot) & 7) * 128);
            STEP(3, 1);
            ADDR(2);
        }
        STAMP(2);
        if (cq == 3) {
            lds_wait(fl + FL_DONE + 5, 8);
#pragma unroll
            for (int et = 0; et < 2; ++et) {
                *(LAS u32x4*)((lds_ptr)lds + SP_Q + (2 * th + et) * 1024 + lane * 16) = pack8(acc[1][et], 0, QSCALE);
                *(LAS u32x4*)((lds_ptr)lds + SP_Q + 4096 + (2 * th + et) * 1024 + lane * 16) = pack8(acc[1][et], 8, QSCALE);
            }
            lds_sig(fl + FL_QDONE);
        }
        lds_wait(fl + FL_GDONE, 8);
        const LAS float* gl = (const LAS float*)((lds_ptr)lds + SP_G);
#define GATE(t) __builtin_amdgcn_rcpf(1.0f + __expf(-(gl[t] + gl[128 + (t)] + gl[256 + (t)] + gl[384 + (t)])))
        lds_wait(fl + FL_DONE + 7, 8);
#pragma unroll
        for (int et = 0; et < 2; ++et) {
            const float om = 1.0f - GATE(64 * th + 32 * et + r32);
#pragma unroll
            for (int e = 0; e < 2; ++e) {
                if (e == 1 && cq == 3) continue;
                const f32x16 a = acc[e][et];
                *(LAS u32x4*)((lds_ptr)lds + SP_H + ((th * 7 + 2 * cq + e) * 2 + et) * 1024 + lane * 16) =
                    u32x4{cvtpk(a[0] * a[8] * om, a[1] * a[9] * om), cvtpk(a[2] * a[10] * om, a[3] * a[11] * om),
                          cvtpk(a[4] * a[12] * om, a[5] * a[13] * om), cvtpk(a[6] * a[14] * om, a[7] * a[15] * om)};
            }
        }
        lds_sig(fl + FL_HCNT);
        STAMP(3);
        const int tbl = wid & 3, eh = wid >> 2;
        lds_wait(fl + FL_QDONE, 2);
        const u32x4 qraw = *(const LAS u32x4*)((lds_cptr)lds + SP_Q + eh * 4096 + tbl * 1024 + lane * 16);
        const float gate = GATE(32 * tbl + r32);
        const float qn2 = xhalf_sum(norm2_8(qraw));
        lds_wait(fl + FL_KVFULL, 2);
        STAMP(4);
        const float kmax2 = __uint_as_float(lds_ld(fl + FL_KMAX));
        const float bound = __builtin_amdgcn_sqrtf(qn2 * kmax2) * 1.001f + 1e-6f;
        const bool fast = __all(bound <= 40.0f);
        const bf16x8 qf = __builtin_bit_cast(bf16x8, qraw);
        const lds_cptr kp = (lds_cptr)lds + KVOFF + lane * 16;
        const lds_cptr vp = (lds_cptr)lds + KVOFF + 1024 + (r32 < 16 ? (hi * 16 + r32) * 16 : 512);
        f32x16 o = {};
        if (eh == 1) __builtin_amdgcn_s_setprio(1);
        if (fast) {
            f32x16 cneg;
#pragma unroll
            for (int i = 0; i < 16; ++i) cneg[i] = -bound;
            PIN(cneg);
#define EX(S, i) S[i] = __builtin_amdgcn_exp2f(S[i])
#define TILE(SC, SP, OFF, FIRST, QK, KLD, VLD)                                                                     \
            {                                                                                                      \
                EX(SC, 0); EX(SC, 1); PIN(SC); SBAR();                                                             \
                if (!(FIRST)) { o = SMF(v1, hi8(SP), o); SBAR(); v1 = lds_frag(vq + (OFF) + 528); SBAR(); }        \
                EX(SC, 2); EX(SC, 3); EX(SC, 4); EX(SC, 5); EX(SC, 6); PIN(SC); SBAR();                            \
                if (QK) { SP = MFMA32(kn, qf, cneg); SBAR(); }                                                     \
                if (KLD) { kn = lds_frag(kq + (OFF) + 2 * KVREC); SBAR(); }                                        \
                EX(SC, 7); EX(SC, 8); EX(SC, 9); EX(SC, 10); EX(SC, 11); PIN(SC); SBAR();                          \
                o = SMF(v0, lo8(SC), o); SBAR();                                                                   \
                if (VLD) { v0 = lds_frag(vq + (OFF) + KVREC); SBAR(); }                                            \
                EX(SC, 12); EX(SC, 13); EX(SC, 14); EX(SC, 15); PIN(SC); SBAR();                                   \
            }
#define TILE4() do { TILE(sA, sB, 0, false, true, true, true); TILE(sB, sA, KVREC, false, true, true, true);      \
                     TILE(sA, sB, 2 * KVREC, false, true, true, true); TILE(sB, sA, 3 * KVREC, false, true, true, true); \
                     kq += 4 * KVREC; vq += 4 * KVREC; } while (0)
            lds_cptr kq = kp, vq = vp;
            f32x16 sA = MFMA32(lds_frag(kq), qf, cneg), sB;
            bf16x8 kn = lds_frag(kq + KVREC), v0 = lds_frag(vq), v1 = lds_frag(vq + 528);
            TILE(sA, sB, 0, true, true, true, true);
            TILE(sB, sA, KVREC, false, true, true, true);
            kq += 2 * KVREC; vq += 2 * KVREC;
#pragma nounroll
            for (int seg = 0; seg < 5; ++seg) {
                if (seg == 1) lds_wait(fl + FL_KVFULL + 1, 2);
                if (seg == 2) __builtin_amdgcn_s_setprio(0);
                if (seg >= 3) lds_wait(fl + FL_KVFULL + seg - 1, 2);
#pragma nounroll
                for (int n = ((0x23004 | (PSWITCH << 4) | ((6 - PSWITCH) << 8)) >> (4 * seg)) & 15; n > 0; --n) TILE4();
            }
            TILE(sA, sB, 0, false, true, false, true);
            TILE(sB, sA, KVREC, false, false, false, false);
            o = SMF(v1, hi8(sB), o);
        } else {
            lds_wait(fl + FL_KVFULL + 1, 2);
            lds_wait(fl + FL_KVFULL + 2, 2);
            lds_wait(fl + FL_KVFULL + 3, 2);
            float m = -__builtin_inff();
            const f32x16 zero = {};
            for (int t = 0; t < 64; ++t) {
                f32x16 s = MFMA32(lds_frag(kp + t * KVREC), qf, zero);
                const bf16x8 v0 = lds_frag(vp + t * KVREC), v1 = lds_frag(vp + t * KVREC + 528);
                float tm = s[0];
#pragma unroll
                for (int i = 1; i < 16; ++i) tm = __builtin_fmaxf(tm, s[i]);
                tm = xhalf_max(tm);
                const float mn = __builtin_fmaxf(m, tm);
                const float alpha = __builtin_amdgcn_exp2f(m - mn);
                m = mn;
#pragma unroll
                for (int i = 0; i < 16; ++i) { o[i] *= alpha; s[i] = __builtin_amdgcn_exp2f(s[i] - mn); }
                o = SMF(v0, lo8(s), o);
                o = SMF(v1, hi8(s), o);
            }
        }
        STAMP(5);
        const float sc = gate * __builtin_amdgcn_rcpf(o[8]);
        ST16(pack8(o, 0, sc), WS_AF + (((2 * tt + (tbl >> 1)) * KB_ALL + KB_H + 2 * ct + eh) * 2 + (tbl & 1)) * 1024 + lane * 16);
        VMCNT(0);
        if (lds_sig_ret(fl + FL_OCNT + eh) == 3u) { const unsigned long long E_ = get_nonce(fl); FLAG_SET(WS_FO + ((2 * tt + eh) * 16 + ct) * 8, E_); }
        STAMP(6);
        STAMP_FLUSH();
        return;
    }
    __builtin_amdgcn_s_setprio(2);
    const int aw = wid - 8;
    const bool kvtask = ((ct + 1) & 3) < 2;
    const int lh = aw & 1;
    const unsigned xvo0 = (lane >> 3) * 1024 + (((lane & 7) ^ (lane >> 4)) << 4), xvo1 = (xvo0 + 8192) ^ 64;
    const unsigned gvo = (lane >> 4) * (2 * F_) + 224 * ct + 32 * min(((lane >> 1) & 7) ^ (2 * (lane >> 4)), 6) + 16 * (lane & 1);
    const unsigned uvo = (lane >> 4) * (2 * F_) + 224 * ct + 32 * min(((lane >> 1) & 7) ^ (2 * (lane >> 4)) ^ 1, 6) + 16 * (lane & 1);
    const unsigned qvo = ((lane >> 4) * 8 + ((lane >> 2) & 3)) * (2 * D_) + 64 * ct + 16 * (lane & 3);
    auto stq = [&](int c, int sub) {
        const int kc = (c + rot) & 7, qd = 2 * lh + sub;
        const unsigned sb = lds0 + ((c + 1) % 3) * STAGE + qd * 4096;
        const char* xb = (const char*)x + (size_t)(tt * 128 + 32 * qd) * 1024 + kc * 128;
        const size_t wo_ = (size_t)(kc * 64 + 16 * qd) * (2 * F_);
#pragma unroll
        for (int i = 0; i < 4; ++i) {
            glds16s(xb + (i >> 1) * 16384, (i & 1) ? xvo1 : xvo0, sb + i * 1024);
            glds16s((const char*)wg + wo_ + i * 8 * F_, gvo, sb + GB + i * 1024);
            glds16s((const char*)wu + wo_ + i * 8 * F_, uvo, sb + UB + i * 1024);
        }
        glds16s((const char*)wq + (size_t)(kc * 64 + 32 * lh + 4 * sub) * (2 * D_), qvo, sb + QB - qd * 3072);
    };
    if (aw < 2) {
        glds16s(gw, lane * 16, lds0 + SP_GW);
        int pend = 0;
#pragma nounroll
        for (int j = 0; j < 14; ++j) {
            const int c = j < 4 ? j >> 1 : (j >> 1) + 1, sub = j & 1;
            if (j == 1 && !kvtask) continue;
            if (j == 2) asm volatile("s_barrier" ::: "memory");
            if (j >= 4 && sub == 0) {
                if ((unsigned)__builtin_amdgcn_readfirstlane(lds_ld(fl + FL_DONE + c - 3)) < 8u) {
                    VMCNT(0); lds_sig2(fl + FL_FULL + pend); pend = -1;
                    lds_wait(fl + FL_DONE + c - 3, 8);
                }
                CBAR();
            }
            stq(c, sub);
            if (j >= 2 && sub == 0 && pend >= 0) {
                VMCNT(13);
                if (j == 2 && !kvtask) lds_sig(fl + FL_FULL); else lds_sig2(fl + FL_FULL + pend);
            }
            if (sub == 1) pend = c;
        }
        VMCNT(0); lds_sig2(fl + FL_FULL + 7);
        STAMP(1);
        {
            u32x4 hv[14];
            lds_wait(fl + FL_HCNT, 8);
#pragma unroll
            for (int i = 0; i < 14; ++i) hv[i] = *(const LAS u32x4*)((lds_cptr)lds + SP_H + (aw * 14 + i) * 1024 + lane * 16);
            lds_sig(fl + FL_HREAD);
            const int hoff = WS_AF + ((2 * tt + aw) * KB_ALL + 7 * ct) * 2048 + lane * 16;
#pragma unroll
            for (int i = 0; i < 14; ++i) ST16(hv[i], hoff + i * 1024);
            VMCNT(0);
            if (lds_sig_ret(fl + FL_HST) == 1u) { const unsigned long long E_ = get_nonce(fl); FLAG_SET(WS_FH + (tt * 16 + ct) * 8, E_); }
        }
        STAMP(2);
    } else {
        const int kh = aw - 2;
        const int tb = 4 * tt + (ct >> 2), isv = ct & 1;
        const unsigned long long chal = __builtin_amdgcn_s_memrealtime();
        if (aw == 3) FLAG_SET(WS_CHAL + b * 8, chal);
        if (!kvtask) { stq(0, 1); asm volatile("s_barrier" ::: "memory"); }
        if (b == LEADER && aw == 3) {
            if (lane == 0) { __hip_atomic_store(fl + FL_E, (unsigned)chal, RLX_WG); __hip_atomic_store(fl + FL_E + 1, (unsigned)(chal >> 32), RLX_WG); }
            lds_sig(fl + FL_GO);
        }
        const int q4 = (lane & 15) >> 2, p4 = lane & 3;
        f32x16 acc = {};
        if (kvtask) {
            const char* xb = (const char*)x + (size_t)(32 * tb) * 1024 + 512 * kh;
#pragma unroll
            for (int i = 0; i < 16; ++i) {
                glds16s(xb + i * 2048, hi * 1024 + ((r32 ^ ((2 * i + hi) & 15)) << 4), lds0 + kh * 16384 + i * 1024);
            }
            const char* wb = (const char*)(isv ? wv : wk) + 8192 * kh;
#pragma unroll
            for (int i = 0; i < 8; ++i) glds16s(wb + i * 1024, lane * 16, lds0 + 32768 + kh * 8192 + i * 1024);
            asm volatile("s_barrier" ::: "memory");
            VMCNT(0);
            const lds_cptr xl = (lds_cptr)lds + kh * 16384 + r32 * 512, wl = (lds_cptr)lds + 32768 + kh * 8192 + (8 * hi + q4) * 32 + p4 * 8;
#pragma unroll
            for (int ks = 0; ks < 16; ++ks) {
                const bf16x8 xk = lds_frag(xl + (((2 * ks + hi) ^ (r32 & 15)) << 4)), wf = tr_frag(wl + ks * 512, wl + ks * 512 + 128);
                const bf16x8 ma = isv ? xk : wf, mb = isv ? wf : xk;
                acc = MFMA32(ma, mb, acc);
            }
            LAS f32x4* red = (LAS f32x4*)((lds_ptr)lds + SP_RED) + lane;
            if (kh == 1) {
#pragma unroll
                for (int g = 0; g < 4; ++g) red[g * 64] = f32x4{acc[4 * g], acc[4 * g + 1], acc[4 * g + 2], acc[4 * g + 3]};
                lds_sig(fl + FL_KVRED);
                lds_wait(fl + FL_KVSLOT, 1);
            } else {
                lds_wait(fl + FL_KVRED, 1);
#pragma unroll
                for (int g = 0; g < 4; ++g) { const f32x4 p = red[g * 64]; acc[4 * g] += p[0]; acc[4 * g + 1] += p[1]; acc[4 * g + 2] += p[2]; acc[4 * g + 3] += p[3]; }
                PIN(acc);
                lds_sig(fl + FL_KVSLOT);
            }
        }
#pragma nounroll
        for (int sub = 0; sub < 2; ++sub) {
            stq(2, sub);
            if (!kvtask && sub == 0) { VMCNT(13); lds_sig(fl + FL_FULL); }
        }
        VMCNT(0);
        lds_sig2(fl + FL_FULL + 2);
        STAMP(1);
        unsigned long long E;
        if (aw == 3) {
            E = chal;
            if (b != LEADER) {
#pragma nounroll
                for (unsigned s = 0; s < SPIN_CAP; ++s) {
                    CBAR();
                    const u32x4 r = __builtin_amdgcn_raw_buffer_load_b128(rs, WS_RESP + b * 16, 0, 16);
                    E = (unsigned long long)r[2] | ((unsigned long long)r[3] << 32);
                    if (__all(((unsigned long long)r[0] | ((unsigned long long)r[1] << 32)) == (chal ^ E))) break;
                    __builtin_amdgcn_s_sleep(24);
                }
            }
            if (b != LEADER) {
                if (lane == 0) { __hip_atomic_store(fl + FL_E, (unsigned)E, RLX_WG); __hip_atomic_store(fl + FL_E + 1, (unsigned)(E >> 32), RLX_WG); }
                lds_sig(fl + FL_GO);
            }
            FLAG_SET(WS_ACK + b * 8, E);
            STAMP(12);
            if (b == LEADER) {
#pragma nounroll
                for (unsigned s = 0; s < SPIN_CAP; ++s) {
                    if (echo_round(rs, lane, E)) break;
                    __builtin_amdgcn_s_sleep(32);
                }
            }
            STAMP(2);
            lds_wait(fl + FL_KVOK, 1);
        } else {
            if (kvtask) {
                const int rec = WS_KV + tb * KVREC;
                if (isv) {
                    if (r32 < 16) {
#pragma unroll
                        for (int g = 0; g < 4; ++g) {
                            const u32x2 v = {cvtpk(acc[4 * g], acc[4 * g + 1]), cvtpk(acc[4 * g + 2], acc[4 * g + 3])};
                            ST8(v, rec + 1024 + (g >> 1) * 528 + ((g & 1) * 16 + r32) * 16 + hi * 8);
                        }
                    } else if (r32 == 16) {
                        const u32x4 ones = {0x3f803f80u, 0x3f803f80u, 0x3f803f80u, 0x3f803f80u};
                        ST16(ones, rec + 1024 + 512 + hi * 528);
                    }
                } else {
                    const u32x4 kv = pack8n(acc, 0);
                    ST16(kv, rec + lane * 16);
                    float n2 = xhalf_sum(norm2_8(kv));
#pragma unroll
                    for (int of = 1; of < 32; of <<= 1) n2 = __builtin_fmaxf(n2, __shfl_xor(n2, of));
                    if (lane == 0) __hip_atomic_store((gu32*)(ws + WS_KMAX) + tb, __float_as_uint(n2), RLX_AGENT);
                }
                VMCNT(0);
            }
            E = get_nonce(fl);
            if (kvtask) FLAG_SET(WS_FKV + (2 * tb + isv) * 8, E);
            STAMP(2);
            lds_wait(fl + FL_DONE + 4, 8);
#pragma nounroll
            for (unsigned s = 0; s < SPIN_CAP; ++s) {
                CBAR();
                const u32x4 f = __builtin_amdgcn_raw_buffer_load_b128(rs, WS_FKV + lane * 16, 0, 16);
                if (__all(f[0] == (unsigned)E && f[2] == (unsigned)E && f[1] == (unsigned)(E >> 32) && f[3] == (unsigned)(E >> 32))) break;
                __builtin_amdgcn_s_sleep(4);
            }
            CBAR();
            lds_sig(fl + FL_KVOK);
            asm volatile("s_mov_b32 m0, %2\n\ts_nop 0\n\tglobal_load_lds_dword %0, %1 sc1" :: "v"(lane * 4), "s"(ws + WS_KMAX), "s"(lds0 + SP_KMAX) : "memory");
        }
        STAMP(3);
#pragma nounroll
        for (int p = 0; p < 4; ++p) {
            if (p < 3) lds_wait(fl + FL_DONE + 5 + p, 8); else lds_wait(fl + FL_HREAD, 2);
            const int n = (0x050c1a16 >> (8 * p)) & 255, u0 = ((0x78602c00 >> (8 * p)) & 255) + n * kh;
            int i = 0;
#pragma nounroll
            for (; i + 4 <= n; i += 4) glds16x4_sc1(ws + WS_KV + (u0 + i) * 1024, lane * 16, lds0 + KVOFF + (u0 + i) * 1024);
#pragma nounroll
            for (; i < n; ++i) glds16x1_sc1(ws + WS_KV + (u0 + i) * 1024, lane * 16, lds0 + KVOFF + (u0 + i) * 1024);
            if (p == 0 && aw == 2) {
                VMCNT(22);
                float km = ((const LAS float*)((lds_cptr)lds + SP_KMAX))[lane];
#pragma unroll
                for (int of = 1; of < 64; of <<= 1) km = __builtin_fmaxf(km, __shfl_xor(km, of));
                if (lane == 0) __hip_atomic_store(fl + FL_KMAX, __float_as_uint(km), RLX_WG);
            }
            if (p == 1) { VMCNT(26); lds_sig(fl + FL_KVFULL); }
            if (p == 2) { VMCNT(12); lds_sig(fl + FL_KVFULL + 1); }
            if (p >= 2) { VMCNT(0); lds_sig(fl + FL_KVFULL + p); }
        }
        if (aw == 3) {
            const int rp0 = (b & 31) * 9, rpb = (rp0 + 1) >> 1, rpe = (rp0 + 10) >> 1;
#pragma unroll
            for (int i = 0; i < 5; ++i) {
                const int kb = min(rpb + i, rpe - 1);
                const char* src = (const char*)(kb < KB_H ? wd + (size_t)kb * 16 * D_ : wo + (size_t)(kb - KB_H) * 16 * D_) + 128 * (b >> 5);
                glds16s(src, (lane >> 3) * 1024 + (lane & 7) * 16, lds0 + SP_RT + i * 2048);
                glds16s(src + 8192, (lane >> 3) * 1024 + (lane & 7) * 16, lds0 + SP_RT + i * 2048 + 1024);
            }
            VMCNT(0);
            const lds_cptr tb0 = (lds_cptr)lds + SP_RT + (4 * hi + ((lane & 15) >> 2)) * 128 + ((lane >> 4) & 1) * 32 + (lane & 3) * 8;
#pragma unroll
            for (int i = 0; i < 5; ++i) {
                const int kb = min(rpb + i, rpe - 1);
#pragma unroll
                for (int en = 0; en < 2; ++en)
                    ST16(__builtin_bit_cast(u32x4, tr_frag(tb0 + i * 2048 + en * 64, tb0 + i * 2048 + en * 64 + 1024)), WS_WF + (((b >> 5) * KB_ALL + kb) * 2 + en) * 1024 + lane * 16);
            }
            VMCNT(0);
            FLAG_SET(WS_FW + b * 8, E);
        }
        STAMP(4);
    }
    const int tp = 4 * xcd + (idx >> 3), np = idx & 7;
    const int abase = WS_AF + tp * KB_ALL * 2048 + lane * 16, wbase = WS_WF + np * KB_ALL * 2048 + lane * 16;
    const int rot2 = (7 * (np & 3) + 2 * (idx >> 3)) % 28;
    f32x16 acc[2][2] = {};
    constexpr int WIN = 5;
    bf16x8 fa[WIN][2], fw[WIN][2];
    auto kbof = [&](int s) { int k = s + rot2; k = k >= 28 ? k - 28 : k; return (28 * aw + k) * 2048; };
    __builtin_amdgcn_s_setprio(BPRIO);
    const unsigned long long E = get_nonce(fl);
    glb_wait<8>((gu64*)(ws + WS_FW) + np * 32 + r32, E);
#pragma unroll
    for (int s = 0; s < WIN; ++s) { const int o = kbof(s); fw[s][0] = LD16(wbase + o); fw[s][1] = LD16(wbase + o + 1024); }
    glb_wait<8>((gu64*)(ws + WS_FH) + (tp >> 1) * 16 + (lane & 15), E);
    STAMP(5);
#pragma unroll
    for (int s = 0; s < WIN; ++s) { const int o = kbof(s); fa[s][0] = LD16(abase + o); fa[s][1] = LD16(abase + o + 1024); }
    SBAR();
#define GAP() do { SBAR(); __builtin_amdgcn_s_sleep(1); SBAR(); } while (0)
#define MM4S(S) do { acc[0][0] = MFMA32(fw[S][0], fa[S][0], acc[0][0]); GAP(); acc[0][1] = MFMA32(fw[S][0], fa[S][1], acc[0][1]); GAP(); \
                    acc[1][1] = MFMA32(fw[S][1], fa[S][1], acc[1][1]); GAP(); acc[1][0] = MFMA32(fw[S][1], fa[S][0], acc[1][0]); } while (0)
#define MM4(S) do { acc[0][0] = MFMA32(fw[S][0], fa[S][0], acc[0][0]); acc[0][1] = MFMA32(fw[S][0], fa[S][1], acc[0][1]); \
                    acc[1][0] = MFMA32(fw[S][1], fa[S][0], acc[1][0]); acc[1][1] = MFMA32(fw[S][1], fa[S][1], acc[1][1]); } while (0)
#pragma unroll
    for (int s = 0; s < 28; ++s) {
        if (BSPREAD) MM4S(s % WIN); else MM4(s % WIN);
        SBAR();
        if (s + WIN < 28) {
            const int o = kbof(s + WIN);
            fw[s % WIN][0] = LD16(wbase + o); fw[s % WIN][1] = LD16(wbase + o + 1024);
            fa[s % WIN][0] = LD16(abase + o); fa[s % WIN][1] = LD16(abase + o + 1024);
            SBAR();
        }
    }
    STAMP(6);
#pragma unroll
    for (int e = 0; e < 2; ++e) {
#pragma unroll
        for (int s = 0; s < 4; ++s) { const int o = (KB_H + 2 * (4 * aw + s) + e) * 2048; fw[s][0] = LD16(wbase + o); fw[s][1] = LD16(wbase + o + 1024); }
        glb_wait<2>((gu64*)(ws + WS_FO) + (2 * (tp >> 1) + e) * 16 + (lane & 15), E);
        STAMP(7 + 2 * e);
#pragma unroll
        for (int s = 0; s < 4; ++s) { const int o = (KB_H + 2 * (4 * aw + s) + e) * 2048; fa[s][0] = LD16(abase + o); fa[s][1] = LD16(abase + o + 1024); }
        SBAR();
#pragma unroll
        for (int s = 0; s < 4; ++s) MM4(s);
        SBAR();
        STAMP(8 + 2 * e);
    }
    asm volatile("s_barrier" ::: "memory");
    {
        lds_ptr mine = (lds_ptr)lds + aw * RED_TILE;
#pragma unroll
        for (int en = 0; en < 2; ++en)
#pragma unroll
            for (int et = 0; et < 2; ++et)
#pragma unroll
                for (int g = 0; g < 4; ++g)
                    *(LAS f32x4*)(mine + (32 * et + r32) * RED_ROW + (32 * en + 8 * g + 4 * hi) * 4) =
                        f32x4{acc[en][et][4 * g], acc[en][et][4 * g + 1], acc[en][et][4 * g + 2], acc[en][et][4 * g + 3]};
    }
    asm volatile("s_waitcnt lgkmcnt(0)\n\ts_barrier" ::: "memory");
#pragma unroll
    for (int h = 0; h < 2; ++h) {
        const int tok = 16 * aw + 8 * h + (lane >> 3), oc = lane & 7;
        f32x4 s0 = {}, s1 = {};
#pragma unroll
        for (int w = 0; w < 4; ++w) {
            const lds_cptr p = (lds_cptr)lds + w * RED_TILE + tok * RED_ROW + oc * 32;
            s0 += *(const LAS f32x4*)p; s1 += *(const LAS f32x4*)(p + 16);
        }
        const u32x4 v = {cvtpk(s0[0], s0[1]), cvtpk(s0[2], s0[3]), cvtpk(s1[0], s1[1]), cvtpk(s1[2], s1[3])};
        *(u32x4*)(out + (size_t)(64 * tp + tok) * D_ + 64 * np + 8 * oc) = v;
    }
    STAMP(11);
    STAMP_FLUSH();
}

extern "C" void kernel_launch(void* const* d_in, const int* in_sizes, int n_in, void* d_out, int out_size, void* d_ws,
                              size_t ws_size, hipStream_t stream) {
    fused<<<256, 768, 0, stream>>>((const u16*)d_in[0], (const u16*)d_in[1], (const u16*)d_in[2], (const u16*)d_in[3], (const u16*)d_in[4],
                                   (const u16*)d_in[6], (const u16*)d_in[7], (const u16*)d_in[8], (const u16*)d_in[9], (char*)d_ws, (u16*)d_out);
}
```

```cpp
#include <hip/hip_runtime.h>
#include <hip/hip_bf16.h>
#include <stdint.h>

typedef __attribute__((ext_vector_type(2))) __bf16 bf16x2;
typedef __attribute__((ext_vector_type(8))) __bf16 bf16x8;
typedef __attribute__((ext_vector_type(16))) __bf16 bf16x16;
typedef __attribute__((ext_vector_type(16))) float f32x16;
typedef __attribute__((ext_vector_type(8))) float f32x8;
typedef __attribute__((ext_vector_type(4))) float f32x4;
typedef __attribute__((ext_vector_type(4))) unsigned u32x4;
typedef __attribute__((ext_vector_type(2))) unsigned u32x2;
typedef short v4i16_t __attribute__((ext_vector_type(4)));
typedef short v8i16_t __attribute__((ext_vector_type(8)));
typedef unsigned short u16;
#define LAS __attribute__((address_space(3)))
#define GAS __attribute__((address_space(1)))
typedef LAS const char* lds_cptr;
typedef LAS char* lds_ptr;
typedef LAS unsigned lu32;
typedef GAS unsigned long long gu64;

constexpr int D_ = 512, F_ = 1792, NH = 32;
constexpr int KB_H = F_ / 16;
constexpr int KB_ALL = KB_H + NH;
constexpr float QSCALE = 0.25f * 1.4426950408889634f;

constexpr int WS_CHAL = 0, WS_ACK = 2048, WS_RESP = 4096;
constexpr int WS_FKV = 53248, WS_FH = 9216, WS_FW = 11264, WS_FO = 16384;
constexpr int WS_KMAX = 32768, WS_G = 36864, WS_OM = WS_G + 8192;
constexpr int KVREC = 2080, KVIMG = 64 * KVREC;
constexpr int WS_KV = 65536;
constexpr int WS_AF = 262144;
constexpr int WS_WF = WS_AF + 32 * KB_ALL * 2048;
constexpr int WS_END = WS_WF + 8 * KB_ALL * 2048;
constexpr int LEADER = 0;

constexpr int GB = 16384, QB = 49152, STAGE = 53248, RING = 3 * STAGE;
constexpr int SP_FLAG = RING, SP_KMAX = RING + 256, SP_RED8 = RING + 512, SP_GW = RING + 1024, SP_G = RING + 2048, SP_QN = RING + 3072, LDS_BYTES = RING + 4096;
constexpr int SP_Q = 0, SP_RT = 8192, KVOFF = 18432, SP_H = RING - 32768;
enum { FL_HREAD = 0, FL_HST = 1, FL_ECHO = 2, FL_DGO = 3, FL_PAST = 4, FL_COMB = 5, FL_C1 = 7, FL_DONE = 8, FL_KV1 = 17, FL_GO = 20, FL_KVOK = 21, FL_HCNT = 24, FL_OCNT = 25, FL_KVRED = 27, FL_KVSLOT = 29, FL_FULL = 32,
       FL_QDONE = 44, FL_KV0 = 45, FL_KMAX = 46, FL_REST = 48, FL_E = 50 };

#define MFMA32(a, b, c) __builtin_amdgcn_mfma_f32_32x32x16_bf16(a, b, c, 0, 0, 0)
#define SMF(a, p8, c) __builtin_amdgcn_smfmac_f32_32x32x32_bf16(a, __builtin_bit_cast(bf16x16, p8), c, 0xDDDDDDDD, 0, 0)
#define SBAR() __builtin_amdgcn_sched_barrier(0)
#define PIN(x) asm volatile("" : "+v"(x))
#define USE(x) asm volatile("" :: "v"(x))
#define CBAR() asm volatile("" ::: "memory")
#define RLX_AGENT __ATOMIC_RELAXED, __HIP_MEMORY_SCOPE_AGENT
#define RLX_WG __ATOMIC_RELAXED, __HIP_MEMORY_SCOPE_WORKGROUP
#define VMCNT(n) asm volatile("s_waitcnt vmcnt(" #n ")" ::: "memory")

__device__ __forceinline__ void glds16s(const void* sbase, unsigned voff, unsigned lds_base) {
    asm volatile("s_mov_b32 m0, %2\n\ts_nop 0\n\tglobal_load_lds_dwordx4 %0, %1" :: "v"(voff), "s"(sbase), "s"(lds_base) : "memory");
}
__device__ __forceinline__ void glds16x4_sc1(const void* sbase, unsigned voff, unsigned lds_base) {
    asm volatile("s_mov_b32 m0, %2\n\ts_nop 0\n\tglobal_load_lds_dwordx4 %0, %1 sc1\n\tglobal_load_lds_dwordx4 %0, %1 offset:1024 sc1\n\t"
                 "global_load_lds_dwordx4 %0, %1 offset:2048 sc1\n\tglobal_load_lds_dwordx4 %0, %1 offset:3072 sc1" :: "v"(voff), "s"(sbase), "s"(lds_base) : "memory");
}
__device__ __forceinline__ void glds16x1_sc1(const void* sbase, unsigned voff, unsigned lds_base) {
    asm volatile("s_mov_b32 m0, %2\n\ts_nop 0\n\tglobal_load_lds_dwordx4 %0, %1 sc1" :: "v"(voff), "s"(sbase), "s"(lds_base) : "memory");
}
__device__ __forceinline__ unsigned cvtpk(float lo, float hi) {
    typedef __attribute__((ext_vector_type(2))) float f32x2;
    f32x2 v = {lo, hi};
    return __builtin_bit_cast(unsigned, __builtin_convertvector(v, bf16x2));
}
__device__ __forceinline__ u32x4 pack8(const f32x16& a, int o, float s) {
    return u32x4{cvtpk(a[o] * s, a[o + 1] * s), cvtpk(a[o + 2] * s, a[o + 3] * s), cvtpk(a[o + 4] * s, a[o + 5] * s), cvtpk(a[o + 6] * s, a[o + 7] * s)};
}
__device__ __forceinline__ float addf(float a, float b) { float r; asm("v_add_f32 %0, %1, %2" : "=v"(r) : "v"(a), "v"(b)); return r; }
__device__ __forceinline__ float wave_max_pos(float v) {
    int x = __float_as_int(v);
    x = max(x, __builtin_amdgcn_update_dpp(0, x, 0x111, 0xf, 0xf, true));
    x = max(x, __builtin_amdgcn_update_dpp(0, x, 0x112, 0xf, 0xf, true));
    x = max(x, __builtin_amdgcn_update_dpp(0, x, 0x114, 0xf, 0xf, true));
    x = max(x, __builtin_amdgcn_update_dpp(0, x, 0x118, 0xf, 0xf, true));
    x = max(x, __builtin_amdgcn_update_dpp(0, x, 0x142, 0xa, 0xf, true));
    x = max(x, __builtin_amdgcn_update_dpp(0, x, 0x143, 0xc, 0xf, true));
    return __int_as_float(x);
}
__device__ __forceinline__ bf16x8 lds_frag(lds_cptr p) { return *(const LAS bf16x8*)p; }
__device__ __forceinline__ v4i16_t lds_tr(lds_cptr p) { return __builtin_amdgcn_ds_read_tr16_b64_v4i16((LAS v4i16_t*)p); }
__device__ __forceinline__ bf16x8 tr_frag(lds_cptr p0, lds_cptr p1) {
    v4i16_t a = lds_tr(p0), b = lds_tr(p1);
    v8i16_t r = {a[0], a[1], a[2], a[3], b[0], b[1], b[2], b[3]};
    return __builtin_bit_cast(bf16x8, r);
}
__device__ __forceinline__ float xhalf_sum(float v) {
    auto rr = __builtin_amdgcn_permlane32_swap(__float_as_uint(v), __float_as_uint(v), false, false);
    return __uint_as_float(rr[0]) + __uint_as_float(rr[1]);
}
__device__ __forceinline__ float xhalf_max(float v) {
    auto rr = __builtin_amdgcn_permlane32_swap(__float_as_uint(v), __float_as_uint(v), false, false);
    return __builtin_fmaxf(__uint_as_float(rr[0]), __uint_as_float(rr[1]));
}
__device__ __forceinline__ f32x8 lo8(const f32x16& v) { return __builtin_shufflevector(v, v, 0, 1, 2, 3, 4, 5, 6, 7); }
__device__ __forceinline__ f32x8 hi8(const f32x16& v) { return __builtin_shufflevector(v, v, 8, 9, 10, 11, 12, 13, 14, 15); }
__device__ __forceinline__ float norm2_8(const u32x4& v) {
    float n = 0.f;
#pragma unroll
    for (int i = 0; i < 4; ++i) { const unsigned u = v[i]; n = __builtin_amdgcn_fdot2_f32_bf16(__builtin_bit_cast(bf16x2, u), __builtin_bit_cast(bf16x2, u), n, false); }
    return n;
}
__device__ __forceinline__ unsigned lds_ld(lu32* p) { return __hip_atomic_load(p, RLX_WG); }
__device__ __forceinline__ void lds_sig(lu32* p, unsigned w = 1u) {
    asm volatile("s_mov_b64 exec, 1\n\tds_add_u32 %0, %1\n\ts_mov_b64 exec, -1" :: "v"(p), "v"(w) : "memory");
}
__device__ __forceinline__ unsigned lds_sig_ret(lu32* p) {
    unsigned r;
    asm volatile("s_mov_b64 exec, 1\n\tds_add_rtn_u32 %0, %1, %2\n\ts_waitcnt lgkmcnt(0)\n\ts_mov_b64 exec, -1" : "=&v"(r) : "v"(p), "v"(1u) : "memory");
    return __builtin_amdgcn_readfirstlane(r);
}
#define SPIN(s) _Pragma("nounroll") for (unsigned s = 0; s < (1u << 18); ++s)
struct Sync {
    lu32* fl;
    template <int SLEEP = 1>
    __device__ __forceinline__ void wait(int word, unsigned want) {
        unsigned n, v, t;
        asm volatile("s_mov_b32 %0, 0x40000\n1:\n\tds_read_b32 %1, %3\n\ts_waitcnt lgkmcnt(0)\n\tv_readfirstlane_b32 %2, %1\n\ts_cmp_ge_u32 %2, %4\n\t"
                     "s_cbranch_scc1 2f\n\ts_sleep %5\n\ts_sub_u32 %0, %0, 1\n\ts_cbranch_scc0 1b\n2:"
                     : "=&s"(n), "=&v"(v), "=&s"(t) : "v"(fl + word), "i"(want), "i"(SLEEP) : "memory", "scc");
    }
    template <int SLEEP>
    __device__ __forceinline__ void flags(gu64* p, unsigned long long E) {
        SPIN(s) {
            CBAR();
            if (__all(__hip_atomic_load(p, RLX_AGENT) == E)) break;
            __builtin_amdgcn_s_sleep(SLEEP);
        }
        CBAR();
    }
    __device__ __forceinline__ unsigned long long nonce() {
        wait(FL_GO, 1);
        return (unsigned long long)lds_ld(fl + FL_E) | ((unsigned long long)lds_ld(fl + FL_E + 1) << 32);
    }
};
#define FLAG_SET(off, E) do { if (lane == 0) __hip_atomic_store((gu64*)(ws + (off)), E, RLX_AGENT); } while (0)
#define ST16(v, off) __builtin_amdgcn_raw_buffer_store_b128(v, rs, off, 0, 16)

__device__ __forceinline__ bool echo_round(const __amdgpu_buffer_rsrc_t rs, int lane, unsigned long long E) {
    const unsigned e0 = (unsigned)E, e1 = (unsigned)(E >> 32);
    CBAR();
    const u32x4 c0 = __builtin_amdgcn_raw_buffer_load_b128(rs, WS_CHAL + lane * 32, 0, 16), c1 = __builtin_amdgcn_raw_buffer_load_b128(rs, WS_CHAL + lane * 32 + 16, 0, 16);
    const u32x4 a0 = __builtin_amdgcn_raw_buffer_load_b128(rs, WS_ACK + lane * 32, 0, 16), a1 = __builtin_amdgcn_raw_buffer_load_b128(rs, WS_ACK + lane * 32 + 16, 0, 16);
    if (__all(a0[0] == e0 && a0[1] == e1 && a0[2] == e0 && a0[3] == e1 && a1[0] == e0 && a1[1] == e1 && a1[2] == e0 && a1[3] == e1)) return true;
    ST16((u32x4{c0[0] ^ e0, c0[1] ^ e1, e0, e1}), WS_RESP + lane * 64);
    ST16((u32x4{c0[2] ^ e0, c0[3] ^ e1, e0, e1}), WS_RESP + lane * 64 + 16);
    ST16((u32x4{c1[0] ^ e0, c1[1] ^ e1, e0, e1}), WS_RESP + lane * 64 + 32);
    ST16((u32x4{c1[2] ^ e0, c1[3] ^ e1, e0, e1}), WS_RESP + lane * 64 + 48);
    return false;
}

template <int KIND>
__device__ __forceinline__ void project(char* lds, Sync& sy, int wid, int lane, int cq, int nb) {
    lu32* const fl = sy.fl;
    const unsigned lds0 = (unsigned)(uintptr_t)lds;
    const int r32 = lane & 31, hi = lane >> 5, th = wid & 1, sw = KIND != 4 ? (wid >> 1) & 1 : 0;
    const int q4 = (lane & 15) >> 2, p4 = lane & 3, sel = (lane >> 4) & 1, row = 64 * th + 32 * sw + r32, xd = sw ? -4096 : 4096;
    const int xoff = row * 128 + ((hi ^ ((row >> 1) & 7)) << 4);
    const int woff = GB + sel * 16384 + (8 * hi + q4) * 256 + (((2 * cq) ^ (2 * q4) ^ sel) << 5) + p4 * 8, qoff = QB + hi * 256 + q4 * 64 + sel * 32 + p4 * 8;
    f32x16 acc[2][2] = {}, aq = {};
    bf16x8 xf[4][2]; v4i16_t wa[4][3], wb[4][3];
    unsigned ax, ax1, a0, a1, aqa;
#define LP(a) ((lds_cptr)(uintptr_t)(a))
#define ADDRW() do { a0 = so + woff; a1 = a0 ^ 32; aqa = so + qoff; PIN(a0); PIN(a1); if (KIND != 4) PIN(aqa); } while (0)
#define ADDR(J) do { ax = (so + xoff) ^ (32 * (J)); PIN(ax); if (KIND == 5) { ax1 = ax + xd; PIN(ax1); } } while (0)
#define WF(B, E) __builtin_bit_cast(bf16x8, (v8i16_t{wa[B][E][0], wa[B][E][1], wa[B][E][2], wa[B][E][3], wb[B][E][0], wb[B][E][1], wb[B][E][2], wb[B][E][3]}))
#define RX0(N) xf[N][0] = lds_frag(LP(ax))
#define RX1(N) xf[N][1] = lds_frag(KIND == 5 ? LP(ax1) : LP(ax) + 4096)
#define RW(N, E, H) (H ? wb : wa)[N][E] = lds_tr((E == 2 ? LP(aqa) + ((N) >> 1) * 2048 + ((N) & 1) * 512 : LP(E ? a1 : a0) + (N) * 4096) + (H) * 1024)
#define LOADALL(N) do { RX0(N); RW(N, 0, 0); RW(N, 0, 1); if (KIND != 3) RX1(N); RW(N, 1, 0); RW(N, 1, 1); if (KIND != 4) { RW(N, 2, 0); RW(N, 2, 1); } } while (0)
#define STEP(B, N) do { SBAR();                                                                                            \
        acc[0][0] = MFMA32(WF(B, 0), xf[B][0], acc[0][0]); SBAR(); RX0(N); RW(N, 0, 0); SBAR();                            \
        if (KIND != 3) {                                                                                                   \
            acc[0][1] = MFMA32(WF(B, 0), xf[B][1], acc[0][1]); SBAR(); RW(N, 0, 1); RX1(N); SBAR();                        \
            acc[1][1] = MFMA32(WF(B, 1), xf[B][1], acc[1][1]); SBAR(); RW(N, 1, 0); RW(N, 1, 1); SBAR();                   \
            acc[1][0] = MFMA32(WF(B, 1), xf[B][0], acc[1][0]); SBAR();                                                     \
            if (KIND == 5) { RW(N, 2, 0); RW(N, 2, 1); SBAR(); aq = MFMA32(WF(B, 2), xf[B][0], aq); SBAR(); }              \
        } else {                                                                                                           \
            RW(N, 0, 1); SBAR();                                                                                           \
            acc[1][0] = MFMA32(WF(B, 1), xf[B][0], acc[1][0]); SBAR(); RW(N, 1, 0); RW(N, 1, 1); SBAR();                   \
            aq = MFMA32(WF(B, 2), xf[B][0], aq); SBAR(); RW(N, 2, 0); RW(N, 2, 1); SBAR();                                 \
        } } while (0)
    unsigned so = lds0 + STAGE;
    ADDRW(); ADDR(0);
    if (KIND != 4) { VMCNT(0); lds_sig(fl + FL_FULL); }
    sy.wait(FL_FULL, 4);
    LOADALL(0); ADDR(1); LOADALL(1); ADDR(2);
#pragma nounroll
    for (int c = 0; c < 8; ++c) {
        if (KIND == 4) __builtin_amdgcn_s_setprio(1);
        STEP(0, 2);
        ADDR(3);
        STEP(1, 3);
        lds_sig(fl + FL_DONE + c);
        so = so == lds0 + 2 * STAGE ? lds0 : so + STAGE;
        ADDRW(); ADDR(0);
        const unsigned nf = lds_ld(fl + FL_FULL + c + 1);
        CBAR();
        __builtin_amdgcn_s_setprio(0);
        STEP(2, 0);
        ADDR(1);
        STEP(3, 1);
        if ((unsigned)__builtin_amdgcn_readfirstlane(nf) < 4u) {
            sy.wait(FL_FULL + c + 1, 4);
            ADDR(0); LOADALL(0); ADDR(1); LOADALL(1);
        }
        ADDR(2);
    }
    if (KIND == 4) __builtin_amdgcn_s_setprio(1);
    if (KIND != 4) {
        __builtin_amdgcn_s_setprio(2);
        sy.wait(FL_DONE + 5, 8);
        const int tb = 2 * th + sw;
        const u32x4 q0 = pack8(aq, 0, QSCALE), q1 = pack8(aq, 8, QSCALE);
        *(LAS u32x4*)((lds_ptr)lds + SP_Q + tb * 1024 + lane * 16) = q0;
        *(LAS u32x4*)((lds_ptr)lds + SP_Q + 4096 + tb * 1024 + lane * 16) = q1;
        const float n0 = xhalf_sum(norm2_8(q0)), n1 = xhalf_sum(norm2_8(q1));
        ((LAS float*)((lds_ptr)lds + SP_QN))[lane * 4 + tb] = hi ? n1 : n0;
        lds_sig(fl + FL_QDONE);
        __builtin_amdgcn_s_setprio(0);
    }
    u32x4 hp[2][2];
#pragma unroll
    for (int e = 0; e < 2; ++e)
#pragma unroll
        for (int et = 0; et < (KIND == 3 ? 1 : 2); ++et) {
            const f32x16 a = acc[e][et];
            hp[e][et] = u32x4{cvtpk(a[0] * a[8], a[1] * a[9]), cvtpk(a[2] * a[10], a[3] * a[11]), cvtpk(a[4] * a[12], a[5] * a[13]), cvtpk(a[6] * a[14], a[7] * a[15])};
            PIN(hp[e][et]);
        }
    sy.wait(FL_DONE + 7, 8);
    LAS u32x4* hd = (LAS u32x4*)((lds_ptr)lds + SP_H + (th * nb + 2 * cq) * 2048) + lane;
    hd[64 * sw] = hp[0][0]; hd[128 + 64 * sw] = hp[1][0];
    if (KIND != 3) { hd[64 * (sw ^ 1)] = hp[0][1]; hd[128 + 64 * (sw ^ 1)] = hp[1][1]; }
    lds_sig(fl + FL_HCNT);
}

__global__ void __launch_bounds__(768, 3) fused(const u16* __restrict__ x, const u16* __restrict__ wq, const u16* __restrict__ wk,
                                             const u16* __restrict__ wv, const u16* __restrict__ wo, const u16* __restrict__ wg,
                                             const u16* __restrict__ wu, const u16* __restrict__ wd, const u16* __restrict__ gw,
                                             char* ws, u16* __restrict__ out) {
    __shared__ __attribute__((aligned(256))) char lds[LDS_BYTES];
    const int b = blockIdx.x, xcd = b & 7, idx = b >> 3;
    const int lane = threadIdx.x & 63, wid = __builtin_amdgcn_readfirstlane(threadIdx.x >> 6);
    const int r32 = lane & 31, hi = lane >> 5;
    const int tt = 8 * (xcd & 1) + (idx & 7), ct = 4 * (xcd >> 1) + (idx >> 3);
    const unsigned lds0 = (unsigned)(uintptr_t)lds;
    lu32* const fl = (lu32*)((lds_ptr)lds + SP_FLAG);
    Sync sy = {fl};
    if (wid == 0) fl[lane] = lane == FL_FULL + 8 ? 4u : 0u;
    const bool kvtask = ((ct + 1) & 3) >= 2;
    const int nb = kvtask ? 6 : 8, kb0 = 28 * (ct >> 2) + ((0x140e0800 >> (8 * (ct & 3))) & 255);
    unsigned xvo0 = (lane >> 3) * 1024 + (((lane & 7) ^ (lane >> 4)) << 4), xvo1 = (xvo0 + 8192) ^ 64;
    unsigned gvo = (lane >> 4) * (2 * F_) + 32 * (kb0 + min(((lane >> 1) & 7) ^ (2 * (lane >> 4)), nb - 1)) + 16 * (lane & 1);
    unsigned uvo = (lane >> 4) * (2 * F_) + 32 * (kb0 + min(((lane >> 1) & 7) ^ (2 * (lane >> 4)) ^ 1, nb - 1)) + 16 * (lane & 1);
    unsigned qvo = ((lane >> 4) * 8 + ((lane >> 2) & 3)) * (2 * D_) + 64 * ct + 16 * (lane & 3);
    PIN(xvo0); PIN(xvo1); PIN(gvo); PIN(uvo); PIN(qvo);
    const int rot = 2 * (idx & 3);
    auto stq = [&](int cs, int qd) {
        const unsigned sb = lds0 + ((cs + 1) % 3) * STAGE + qd * 4096;
        const int c = (cs + rot) & 7;
        const size_t wo_ = (size_t)(c * 64 + 16 * qd) * (2 * F_);
        unsigned t0, t1, t2;
#define G1(V, B, STEP) "global_load_lds_dwordx4 " V ", " B "\n\ts_add_u32 m0, m0, " STEP "\n\t"
        asm volatile("s_mov_b32 m0, %12\n\tv_add_u32 %0, 0x4000, %3\n\t"
                     G1("%3", "%8", "0x400") "v_add_u32 %1, 0x4000, %4\n\t" G1("%4", "%8", "0x400") "v_add_u32 %2, 0x3800, %5\n\t"
                     G1("%0", "%8", "0x400") "v_add_u32 %0, 0x7000, %5\n\t" G1("%1", "%8", "0x3400") "v_add_u32 %1, 0xa800, %5\n\t"
                     G1("%5", "%9", "0x400") "s_nop 0\n\t" G1("%2", "%9", "0x400") "v_add_u32 %2, 0x3800, %6\n\t"
                     G1("%0", "%9", "0x400") "v_add_u32 %0, 0x7000, %6\n\t" G1("%1", "%9", "0x3400") "v_add_u32 %1, 0xa800, %6\n\t"
                     G1("%6", "%10", "0x400") "s_nop 0\n\t" G1("%2", "%10", "0x400") "s_nop 0\n\t" G1("%0", "%10", "0x400") "s_nop 0\n\t"
                     "global_load_lds_dwordx4 %1, %10\n\ts_mov_b32 m0, %13\n\ts_nop 0\n\tglobal_load_lds_dwordx4 %7, %11"
                     : "=&v"(t0), "=&v"(t1), "=&v"(t2)
                     : "v"(xvo0), "v"(xvo1), "v"(gvo), "v"(uvo), "v"(qvo), "s"((const char*)x + (size_t)(tt * 128 + 32 * qd) * 1024 + c * 128),
                       "s"((const char*)wg + wo_), "s"((const char*)wu + wo_), "s"((const char*)wq + (size_t)(c * 64 + 32 * (qd >> 1) + 4 * (qd & 1)) * (2 * D_)),
                       "s"(sb), "s"(sb + QB - qd * 3072)
                     : "memory", "scc");
    };
    asm volatile("" :: "s"(x), "s"(wq), "s"(wk), "s"(wv), "s"(wo), "s"(wg), "s"(wu), "s"(wd), "s"(gw), "s"(ws), "s"(out));
    if (wid < 4) stq(0, wid);
    const __amdgpu_buffer_rsrc_t rs = __builtin_amdgcn_make_buffer_rsrc(ws, 0, WS_END, 0x00020000);
    const unsigned long long chal = __builtin_amdgcn_s_memrealtime();
    if (wid == 11) FLAG_SET(WS_CHAL + b * 8, chal);
    asm volatile("s_waitcnt lgkmcnt(0)\n\ts_barrier" ::: "memory");
    auto echo_early = [&](unsigned long long E, int word) {
        const unsigned e0 = (unsigned)E, e1 = (unsigned)(E >> 32);
        const u32x4 c0 = __builtin_amdgcn_raw_buffer_load_b128(rs, WS_CHAL + lane * 16, 0, 16), c1 = __builtin_amdgcn_raw_buffer_load_b128(rs, WS_CHAL + 1024 + lane * 16, 0, 16);
        lds_sig(fl + word);
        ST16((u32x4{c0[0] ^ e0, c0[1] ^ e1, e0, e1}), WS_RESP + lane * 32);
        ST16((u32x4{c0[2] ^ e0, c0[3] ^ e1, e0, e1}), WS_RESP + lane * 32 + 16);
        ST16((u32x4{c1[0] ^ e0, c1[1] ^ e1, e0, e1}), WS_RESP + 2048 + lane * 32);
        ST16((u32x4{c1[2] ^ e0, c1[3] ^ e1, e0, e1}), WS_RESP + 2048 + lane * 32 + 16);
    };
    if (__builtin_expect(b == LEADER && wid >= 4, 0)) {
        if (wid == 4) {
            if (lane == 0) { __hip_atomic_store(fl + FL_E, (unsigned)chal, RLX_WG); __hip_atomic_store(fl + FL_E + 1, (unsigned)(chal >> 32), RLX_WG); }
            lds_sig(fl + FL_GO);
            __builtin_amdgcn_s_sleep(8);
            echo_early(chal, FL_ECHO);
        } else if (wid >= 8) sy.wait(FL_ECHO, 1);
    }
    if (wid >= 8 && !(kvtask && wid >= 10)) {
        if (kvtask) __builtin_amdgcn_s_sleep(16);
        if (wid >= 10) sy.wait(FL_C1, 2);
#pragma nounroll
        for (int i = 0; i < 2; ++i) stq(wid < 10 ? 1 : 2, 2 * (wid & 1) + i);
        if (__builtin_expect(b == LEADER && wid == 9, 0)) { echo_early(sy.nonce(), FL_C1); VMCNT(4); lds_sig(fl + FL_FULL + 1, 2u); }
        else if (wid < 10) lds_sig(fl + FL_C1);
    }
    if (wid < 8) {
        if (wid >= 4) project<4>(lds, sy, wid, lane, (wid >> 1) - (kvtask ? 2 : 0), nb);
        else if (kvtask) project<3>(lds, sy, wid, lane, 2, nb);
        else project<5>(lds, sy, wid, lane, wid >> 1, nb);
        const int tbl = wid & 3, eh = wid >> 2;
        f32x16 o, o1, o2, o3;
        {
            bf16x8 zf = {};
            PIN(zf); o = MFMA32(zf, zf, f32x16{}); PIN(zf); o1 = MFMA32(zf, zf, f32x16{}); PIN(zf); o2 = MFMA32(zf, zf, f32x16{}); PIN(zf); o3 = MFMA32(zf, zf, f32x16{});
            SBAR();
        }
        const lds_cptr kp = (lds_cptr)lds + KVOFF + lane * 16;
        const lds_cptr vp = (lds_cptr)lds + KVOFF + 1024 + (r32 < 16 ? (hi * 16 + r32) * 16 : 512);
        u32x4 qraw[4], f4, qn; bf16x8 kn, v0;
#define OPERANDS() do { CBAR(); f4 = *(volatile LAS u32x4*)(fl + FL_QDONE); CBAR();                                                    \
            qn = *(const LAS u32x4*)((lds_cptr)lds + SP_QN + (eh * 32 + r32) * 16); kn = lds_frag(kp + tbl * KVREC);                        \
            _Pragma("unroll") for (int j = 1; j < 5; ++j) qraw[j & 3] = *(const LAS u32x4*)((lds_cptr)lds + SP_Q + eh * 4096 + (tbl ^ (j & 3)) * 1024 + lane * 16); \
            v0 = lds_frag(vp + tbl * KVREC); CBAR(); } while (0)
        OPERANDS();
        if (__builtin_expect((unsigned)__builtin_amdgcn_readfirstlane(f4[0]) < 4u || (unsigned)__builtin_amdgcn_readfirstlane(f4[1]) < 2u, 0)) {
            sy.wait(FL_QDONE, 4); sy.wait(FL_KV0, 2);
            OPERANDS();
        }
        bf16x8 v1 = v0;
        const float kmax2 = __uint_as_float(f4[2]);
        const float qn2 = __int_as_float(max(max((int)qn[0], (int)qn[1]), max((int)qn[2], (int)qn[3])));
        const float bound = __builtin_amdgcn_sqrtf(qn2 * kmax2) * 1.001f + 1e-6f;
        const bool fast = __all(bound <= 32.0f);
#define QF(j) __builtin_bit_cast(bf16x8, qraw[j])
        float gate; u32x2 En;
#define GETG() do { gate = ((const LAS float*)((lds_cptr)lds + SP_G))[32 * tbl + r32]; En = *(const LAS u32x2*)(fl + FL_E); } while (0)
#define WAIT_REST() sy.wait(FL_REST, 4)
        if (__builtin_expect(fast, 1)) {
            f32x16 cneg;
#pragma unroll
            for (int i = 0; i < 16; ++i) cneg[i] = -bound;
            PIN(cneg);
#define EX(S, i) S[i] = __builtin_amdgcn_exp2f(S[i])
#define UNIT(SC, SP, OC, OP, QN, LD1, LD2, LD3)                                                                    \
            {                                                                                                      \
                EX(SC, 0); EX(SC, 1); USE(SC); SBAR();                                                             \
                OP = SMF(v1, hi8(SP), OP); SBAR(); LD1; SBAR();                                                    \
                EX(SC, 2); EX(SC, 3); EX(SC, 4); USE(SC); SBAR();                                                  \
                if ((QN) >= 0) SP = MFMA32(kn, QF((QN) & 3), cneg);                                                \
                SBAR(); LD2; SBAR();                                                                               \
                EX(SC, 5); EX(SC, 6); EX(SC, 7); USE(SC); SBAR(); EX(SC, 8); EX(SC, 9); EX(SC, 10); USE(SC); SBAR(); \
                OC = SMF(v0, lo8(SC), OC); SBAR();                                                                 \
                EX(SC, 11); EX(SC, 12); USE(SC); SBAR(); LD3; SBAR(); EX(SC, 13); EX(SC, 14); EX(SC, 15); USE(SC); SBAR(); \
            }
            const lds_ptr cb = (lds_ptr)lds + KVOFF + eh * 27648;
#define PUT(O, j) do { const lds_ptr d_ = cb + ((tbl ^ (j)) * 3 + (j) - 1) * 2304;                                             \
                *(LAS f32x4*)(d_ + lane * 16) = f32x4{O[0], O[1], O[2], O[3]}; *(LAS f32x4*)(d_ + 1024 + lane * 16) = f32x4{O[4], O[5], O[6], O[7]}; \
                *(LAS float*)(d_ + 2048 + lane * 4) = O[8]; } while (0)
            lds_cptr kq = kp + tbl * KVREC, vq = vp + tbl * KVREC;
            f32x16 sA = MFMA32(kn, QF(1), cneg), sB = {};
#define KSTEP(OFF, X) do { UNIT(sA, sB, o1, o, 2, v1 = lds_frag(vq + (OFF) + 528), , );                             \
                UNIT(sB, sA, o2, o1, 3, X, , );                                                                    \
                UNIT(sA, sB, o3, o2, 0, , kn = lds_frag(kq + (OFF) + 4 * KVREC), );                                \
                UNIT(sB, sA, o, o3, 1, , , v0 = lds_frag(vq + (OFF) + 4 * KVREC)); } while (0)
#define CHECK(word, want) do { if (__builtin_expect((unsigned)__builtin_amdgcn_readfirstlane(fv) < (want), 0)) sy.wait<2>(word, want); CBAR(); } while (0)
            unsigned fv;
            KSTEP(0 * KVREC, );
            KSTEP(4 * KVREC, );
            KSTEP(8 * KVREC, fv = lds_ld(fl + FL_KV1));
            CHECK(FL_KV1, 2);
            KSTEP(12 * KVREC, );
            KSTEP(16 * KVREC, );
            KSTEP(20 * KVREC, );
            kq += 24 * KVREC; vq += 24 * KVREC; PIN(kq); PIN(vq);
            KSTEP(0 * KVREC, );
            __builtin_amdgcn_s_setprio(0);
            KSTEP(4 * KVREC, );
            KSTEP(8 * KVREC, { lds_sig(fl + FL_PAST); fv = lds_ld(fl + FL_REST); });
            CHECK(FL_REST, 4);
            KSTEP(12 * KVREC, );
            KSTEP(16 * KVREC, );
            KSTEP(20 * KVREC, );
            kq += 24 * KVREC; vq += 24 * KVREC; PIN(kq); PIN(vq);
            KSTEP(0 * KVREC, );
            KSTEP(4 * KVREC, lds_sig(fl + FL_OCNT));
            KSTEP(8 * KVREC, fv = lds_ld(fl + FL_PAST));
            CHECK(FL_PAST, 8);
            UNIT(sA, sB, o1, o, 2, v1 = lds_frag(vq + 12 * KVREC + 528), GETG(), );
            UNIT(sB, sA, o2, o1, 3, , , PUT(o1, 1));
            UNIT(sA, sB, o3, o2, 0, , , PUT(o2, 2));
            UNIT(sB, sA, o, o3, -1, , , { PUT(o3, 3); lds_sig(fl + FL_COMB + eh); });
            o = SMF(v1, hi8(sB), o);
            sy.wait(FL_COMB + eh, 4);
#pragma unroll
            for (int j = 0; j < 3; ++j) {
                const lds_cptr d = cb + (tbl * 3 + j) * 2304;
                const f32x4 p0 = *(const LAS f32x4*)(d + lane * 16), p1 = *(const LAS f32x4*)(d + 1024 + lane * 16);
                o[8] += *(const LAS float*)(d + 2048 + lane * 4);
#pragma unroll
                for (int i = 0; i < 4; ++i) { o[i] += p0[i]; o[4 + i] += p1[i]; }
            }
        } else {
            sy.wait(FL_KV1, 2);
            WAIT_REST();
            GETG();
            float m = -__builtin_inff();
            const f32x16 zero = {};
            for (int t = 0; t < 64; ++t) {
                f32x16 s = MFMA32(lds_frag(kp + t * KVREC), QF(0), zero);
                v0 = lds_frag(vp + t * KVREC); v1 = lds_frag(vp + t * KVREC + 528);
                float tm = s[0];
#pragma unroll
                for (int i = 1; i < 16; ++i) tm = __builtin_fmaxf(tm, s[i]);
                tm = xhalf_max(tm);
                const float mn = __builtin_fmaxf(m, tm);
                const float alpha = __builtin_amdgcn_exp2f(m - mn);
                m = mn;
#pragma unroll
                for (int i = 0; i < 16; ++i) { o[i] *= alpha; s[i] = __builtin_amdgcn_exp2f(s[i] - mn); }
                o = SMF(v0, lo8(s), o);
                o = SMF(v1, hi8(s), o);
                if (t == 36) lds_sig(fl + FL_PAST);
            }
        }
        const float sc = gate * __builtin_amdgcn_rcpf(o[8]);
        u32x4 ov = pack8(o, 0, sc);
#pragma unroll
        for (int i = 0; i < 4; ++i) ov[i] = min(ov[i], 0xfffffffeu);
        ST16(ov, WS_AF + (((2 * tt + (tbl >> 1)) * KB_ALL + KB_H + 2 * ct + eh) * 2 + (tbl & 1)) * 1024 + lane * 16);
        FLAG_SET(WS_FO + (((4 * tt + tbl) * 2 + eh) * 16 + ct) * 8, ((unsigned long long)En[1] << 32 | En[0]));
        if (!fast) lds_sig(fl + FL_OCNT);
        return;
    }
    __builtin_amdgcn_s_setprio(2);
    const int aw = wid - 8;
    const int lh = aw & 1;
    auto kvpart = [&](int p) {
        const int n = (0x0c0a1a11 >> (8 * p)) & 255, u0 = ((0x6a562200 >> (8 * p)) & 255) + n * lh;
        int i = 0;
#pragma nounroll
        for (; i + 4 <= n; i += 4) glds16x4_sc1(ws + WS_KV + (u0 + i) * 1024, lane * 16, lds0 + KVOFF + (u0 + i) * 1024);
#pragma nounroll
        for (; i < n; ++i) glds16x1_sc1(ws + WS_KV + (u0 + i) * 1024, lane * 16, lds0 + KVOFF + (u0 + i) * 1024);
    };
    if (aw < 2) {
        int pend = -1;
        if (!(b == LEADER && aw == 1)) { VMCNT(0); lds_sig(fl + FL_FULL + 1, 2u); }
#pragma nounroll
        for (int j = kvtask ? 0 : 2; j < 8; ++j) {
            const int c = (0x6432 >> (4 * (j >> 1))) & 15, sub = j & 1;
            if (sub == 0) {
                const int fr = j == 0 ? FL_KVSLOT : FL_DONE + c - 3;
                if ((unsigned)__builtin_amdgcn_readfirstlane(lds_ld(fl + fr)) < 8u) {
                    if (pend >= 0) { VMCNT(0); lds_sig(fl + FL_FULL + pend, 2u); pend = -1; }
                    sy.wait(fr, 8);
                }
                CBAR();
            }
            stq(c, 2 * lh + sub);
            if (sub == 0 && pend >= 0) { VMCNT(13); lds_sig(fl + FL_FULL + pend, 2u); }
            if (sub == 1) pend = c;
        }
        VMCNT(0); lds_sig(fl + FL_FULL + 6, 2u);
        if (aw == 0) {
            const unsigned long long E = sy.nonce();
            sy.wait(FL_DONE + 4, 8);
            SPIN(sp) {
                CBAR();
                const u32x4 f = __builtin_amdgcn_raw_buffer_load_b128(rs, lane * 16, WS_FKV + xcd * 1024, 16);
                if (__all(f[0] == (unsigned)E && f[2] == (unsigned)E && f[1] == (unsigned)(E >> 32) && f[3] == (unsigned)(E >> 32))) break;
                __builtin_amdgcn_s_sleep(12);
            }
            CBAR();
            lds_sig(fl + FL_KVOK);
            asm volatile("s_mov_b32 m0, %2\n\ts_nop 0\n\tglobal_load_lds_dword %0, %1 sc1" :: "v"(lane * 4), "s"(ws + WS_KMAX), "s"(lds0 + SP_KMAX) : "memory");
            asm volatile("s_mov_b32 m0, %2\n\ts_nop 0\n\tglobal_load_lds_dword %0, %1 sc1\n\tglobal_load_lds_dword %0, %1 offset:256 sc1" :: "v"(lane * 4), "s"(ws + WS_G + tt * 512), "s"(lds0 + SP_G) : "memory");
        } else sy.wait(FL_KVOK, 1);
#pragma nounroll
        for (int p = 0; p < 2; ++p) {
            sy.wait(FL_DONE + 5 + p, 8);
            kvpart(p);
            if (p == 0 && aw == 0) {
                VMCNT(17);
                const float km = wave_max_pos(((const LAS float*)((lds_cptr)lds + SP_KMAX))[lane]);
                if (lane == 63) __hip_atomic_store(fl + FL_KMAX, __float_as_uint(km), RLX_WG);
            }
            VMCNT(0);
            lds_sig(fl + (p ? FL_KV1 : FL_KV0));
        }
        {
            const u32x4 poison = {~0u, ~0u, ~0u, ~0u};
#pragma unroll
            for (int i = 0; i < 4; ++i) ST16(poison, WS_AF + ((2 * tt + aw) * KB_ALL + KB_H + 2 * ct) * 2048 + i * 1024 + lane * 16);
            u32x4 hv[16];
            sy.wait(FL_HCNT, 8);
#pragma unroll
            for (int i = 0; i < 16; ++i) hv[i] = *(const LAS u32x4*)((lds_cptr)lds + SP_H + (aw * 2 * nb + i) * 1024 + lane * 16);
            lds_sig(fl + FL_HREAD);
            const int hoff = WS_AF + ((2 * tt + aw) * KB_ALL + kb0) * 2048 + lane * 16;
#pragma unroll
            for (int i = 0; i < 16; ++i) if (i < 12 || !kvtask) ST16(hv[i], hoff + i * 1024);
            VMCNT(0);
            lds_sig(fl + FL_REST);
            if (lds_sig_ret(fl + FL_HST) == 1u) { const unsigned long long E_ = sy.nonce(); FLAG_SET(WS_FH + (tt * 16 + ct) * 8, E_); }
        }
    } else {
        const int kh = aw - 2;
        const int tb = 4 * tt + (ct >> 2), isv = ct & 1;
        const int q4 = (lane & 15) >> 2, p4 = lane & 3;
        bf16x8 ma[16], mb[16];
        if (kvtask) {
            const char* xb = (const char*)x + (size_t)(32 * tb) * 1024 + 512 * kh;
#pragma unroll
            for (int i = 0; i < 16; ++i) {
                glds16s(xb + i * 2048, hi * 1024 + ((r32 ^ ((2 * i + hi) & 15)) << 4), lds0 + kh * 16384 + i * 1024);
            }
            const char* wb = (const char*)(isv ? wv : wk) + 8192 * kh;
#pragma unroll
            for (int i = 0; i < 8; ++i) glds16s(wb + i * 1024, lane * 16, lds0 + 32768 + kh * 8192 + i * 1024);
            glds16s(gw, lane * 16, lds0 + SP_GW);
            VMCNT(0);
            const lds_cptr xl = (lds_cptr)lds + kh * 16384 + r32 * 512, wl = (lds_cptr)lds + 32768 + kh * 8192 + (8 * hi + q4) * 32 + p4 * 8;
            if (isv) {
#pragma unroll
                for (int ks = 0; ks < 16; ++ks) { ma[ks] = lds_frag(xl + (((2 * ks + hi) ^ (r32 & 15)) << 4)); mb[ks] = tr_frag(wl + ks * 512, wl + ks * 512 + 128); }
            } else {
#pragma unroll
                for (int ks = 0; ks < 16; ++ks) { mb[ks] = lds_frag(xl + (((2 * ks + hi) ^ (r32 & 15)) << 4)); ma[ks] = tr_frag(wl + ks * 512, wl + ks * 512 + 128); }
            }
            lds_sig(fl + FL_KVSLOT, 4);
            if (!isv && r32 >= 16) {
#pragma unroll
                for (int ks = 0; ks < 16; ++ks) ma[ks] = lds_frag((lds_cptr)lds + SP_GW + kh * 512 + ks * 32 + hi * 16);
            }
            const int rec = WS_KV + tb * KVREC;
            f32x16 acc = {};
#pragma unroll
            for (int ks = 0; ks < 16; ++ks) acc = MFMA32(ma[ks], mb[ks], acc);
            SBAR();
            if (isv) {
                LAS f32x4* red = (LAS f32x4*)((lds_ptr)lds + SP_G) + hi * 16 + (r32 & 15);
                if (kh == 1) {
#pragma unroll
                    for (int g = 0; g < 4; ++g) red[g * 32] = f32x4{acc[4 * g], acc[4 * g + 1], acc[4 * g + 2], acc[4 * g + 3]};
                    lds_sig(fl + FL_KVRED);
                } else {
                    sy.wait(FL_KVRED, 1);
                    unsigned pk[8];
#pragma unroll
                    for (int g = 0; g < 4; ++g) {
                        const f32x4 p = red[g * 32];
                        pk[2 * g] = cvtpk(addf(acc[4 * g], p[0]), addf(acc[4 * g + 1], p[1])); pk[2 * g + 1] = cvtpk(addf(acc[4 * g + 2], p[2]), addf(acc[4 * g + 3], p[3]));
                    }
#pragma unroll
                    for (int g = 0; g < 4; g += 2) {
                        const auto r0 = __builtin_amdgcn_permlane32_swap(pk[2 * g], pk[2 * g + 2], false, false);
                        const auto r1 = __builtin_amdgcn_permlane32_swap(pk[2 * g + 1], pk[2 * g + 3], false, false);
                        const u32x4 v = r32 < 16 ? u32x4{r0[0], r1[0], r0[1], r1[1]} : u32x4{0x3f803f80u, 0x3f803f80u, 0x3f803f80u, 0x3f803f80u};
                        if (r32 < 16 || lane == 16) ST16(v, rec + 1024 + (g >> 1) * 528 + (r32 < 16 ? (hi * 16 + r32) * 16 : 512));
                    }
                }
            } else {
                LAS f32x4* red = (LAS f32x4*)((lds_ptr)lds + SP_G) + lane;
                LAS float* red8 = (LAS float*)((lds_ptr)lds + SP_RED8) + lane;
                if (kh == 1) {
                    red[0] = f32x4{acc[0], acc[1], acc[2], acc[3]}; red[64] = f32x4{acc[4], acc[5], acc[6], acc[7]}; *red8 = acc[8];
                    lds_sig(fl + FL_KVRED);
                } else {
                    sy.wait(FL_KVRED, 1);
                    const f32x4 p0 = red[0], p1 = red[64];
                    const float z = addf(acc[8], *red8);
                    const u32x4 kv = {cvtpk(addf(acc[0], p0[0]), addf(acc[1], p0[1])), cvtpk(addf(acc[2], p0[2]), addf(acc[3], p0[3])),
                                      cvtpk(addf(acc[4], p1[0]), addf(acc[5], p1[1])), cvtpk(addf(acc[6], p1[2]), addf(acc[7], p1[3]))};
                    ST16(kv, rec + lane * 16);
                    const int gq = __float_as_int(__builtin_amdgcn_rcpf(1.0f + __expf(hi ? z : -z)));
                    const u32x4 g4 = {(unsigned)gq, (unsigned)__builtin_amdgcn_mov_dpp(gq, 0x55, 0xf, 0xf, true), (unsigned)__builtin_amdgcn_mov_dpp(gq, 0xaa, 0xf, 0xf, true),
                                      (unsigned)__builtin_amdgcn_mov_dpp(gq, 0xff, 0xf, 0xf, true)};
                    if ((lane & 3) == 0) ST16(g4, WS_G + hi * 8192 + (32 * tb + r32) * 4);
                    const float n2 = wave_max_pos(xhalf_sum(norm2_8(kv)));
                    if (lane == 63) __builtin_amdgcn_raw_buffer_store_b32(__float_as_uint(n2), rs, 0, WS_KMAX + tb * 4, 16);
                }
            }
        }
        unsigned long long E;
        if (aw == 3) {
            if (!kvtask) { VMCNT(0); lds_sig(fl + FL_FULL + 2, 2u); }
            u32x4 r = __builtin_amdgcn_raw_buffer_load_b128(rs, 0, WS_RESP + b * 16, 16);
            unsigned e0, e1;
            if (b == LEADER) { const unsigned long long t = sy.nonce(); e0 = __builtin_amdgcn_readfirstlane((unsigned)t); e1 = __builtin_amdgcn_readfirstlane((unsigned)(t >> 32)); }
            else {
                SPIN(sp) {
                    e0 = __builtin_amdgcn_readfirstlane(r[2]); e1 = __builtin_amdgcn_readfirstlane(r[3]);
                    if ((unsigned)__builtin_amdgcn_readfirstlane(r[0]) == ((unsigned)chal ^ e0) && (unsigned)__builtin_amdgcn_readfirstlane(r[1]) == ((unsigned)(chal >> 32) ^ e1)) break;
                    __builtin_amdgcn_s_sleep(12);
                    CBAR();
                    r = __builtin_amdgcn_raw_buffer_load_b128(rs, 0, WS_RESP + b * 16, 16);
                }
            }
            E = (unsigned long long)e1 << 32 | e0;
            if (b != LEADER) {
                if (lane == 0) { __hip_atomic_store(fl + FL_E, (unsigned)E, RLX_WG); __hip_atomic_store(fl + FL_E + 1, (unsigned)(E >> 32), RLX_WG); }
                lds_sig(fl + FL_GO);
            }
            FLAG_SET(WS_ACK + b * 8, E);
            if (b == LEADER) echo_round(rs, lane, E);
        } else {
            VMCNT(0);
            if (!kvtask) lds_sig(fl + FL_FULL + 2, 2u);
            E = sy.nonce();
            if (kvtask && lane < 8) __hip_atomic_store((gu64*)(ws + WS_FKV) + lane * 128 + 2 * tb + isv, E, RLX_AGENT);
        }
#pragma nounroll
        for (int j = 0; j < 4; ++j) {
            const int c = 5 + (j & 2);
            if (!(j & 1)) sy.wait(FL_DONE + c - 3, 8);
            stq(c, 2 * lh + (j & 1));
            if (j & 1) { VMCNT(0); lds_sig(fl + FL_FULL + c, 2u); }
        }
        if (aw == 3) {
            if (b == LEADER) {
                SPIN(sp) {
                    if (echo_round(rs, lane, E)) break;
                    __builtin_amdgcn_s_sleep(8);
                }
            }
        }
        sy.wait(FL_KVOK, 1);
        const int rp0 = (b & 31) * 9, rpb = (rp0 + 1) >> 1, rpe = (rp0 + 10) >> 1;
#pragma nounroll
        for (int p = 2; p < 4; ++p) {
            if (p == 2) sy.wait(FL_DONE + 7, 8); else sy.wait(FL_HREAD, 2);
            kvpart(p);
            if (p == 2 && aw == 3) {
#pragma unroll
            for (int i = 0; i < 5; ++i) {
                const int kb = min(rpb + i, rpe - 1);
                const char* src = (const char*)(kb < KB_H ? wd + (size_t)kb * 16 * D_ : wo + (size_t)(kb - KB_H) * 16 * D_) + 128 * (b >> 5);
                glds16s(src, (lane >> 3) * 1024 + (lane & 7) * 16, lds0 + SP_RT + i * 2048);
                glds16s(src + 8192, (lane >> 3) * 1024 + (lane & 7) * 16, lds0 + SP_RT + i * 2048 + 1024);
            }
            }
            if (p == 3 || (p == 2 && aw == 3)) VMCNT(0);
            if (p == 3) lds_sig(fl + FL_REST);
            if (p == 2 && aw == 3) {
            const lds_cptr tb0 = (lds_cptr)lds + SP_RT + (4 * hi + ((lane & 15) >> 2)) * 128 + ((lane >> 4) & 1) * 32 + (lane & 3) * 8;
#pragma unroll
            for (int i = 0; i < 5; ++i) {
                const int kb = min(rpb + i, rpe - 1);
#pragma unroll
                for (int en = 0; en < 2; ++en)
                    ST16(__builtin_bit_cast(u32x4, tr_frag(tb0 + i * 2048 + en * 64, tb0 + i * 2048 + en * 64 + 1024)), WS_WF + (((b >> 5) * KB_ALL + kb) * 2 + en) * 1024 + lane * 16);
            }
            }
            if (p == 3 && aw == 3) FLAG_SET(WS_FW + b * 8, E);
        }
    }
    const int tp = 4 * xcd + (idx >> 3), np = idx & 7;
    const int av0 = (aw & 1) * 1024 + lane * 16, av1 = av0 ^ 1024, wv0 = (aw >> 1) * 1024 + lane * 16, wv1 = wv0 ^ 1024;
    const int abase = WS_AF + tp * KB_ALL * 2048, wbase = WS_WF + np * KB_ALL * 2048;
#define LD16S(v, so) __builtin_bit_cast(bf16x8, __builtin_amdgcn_raw_buffer_load_b128(rs, v, so, 16))
    const int rot2 = (7 * (np & 3) + 2 * (idx >> 3)) % 28;
    f32x16 acc[2][2];
    if (aw == 1) {
        const unsigned long long E = sy.nonce();
        sy.flags<8>((gu64*)(ws + (hi ? WS_FW + np * 256 : WS_FH + (tp >> 1) * 128)) + (hi ? r32 : lane & 15), E);
        lds_sig(fl + FL_DGO);
    }
    sy.wait<12>(FL_DGO, 1);
    sy.wait(FL_KVOK, 1);
    {
        constexpr int WIN = 4;
        bf16x8 fa[WIN][2], fw[WIN][2];
        int kn = rot2;
#define LDSTEP(S) do { const int o_ = (28 * aw + kn) * 2048; kn = kn == 27 ? 0 : kn + 1;                                        \
            fw[S][0] = LD16S(wv0, wbase + o_); fa[S][0] = LD16S(av0, abase + o_); fa[S][1] = LD16S(av1, abase + o_); fw[S][1] = LD16S(wv1, wbase + o_); SBAR(); } while (0)
#define MM4S(S) do { acc[0][0] = MFMA32(fw[S][0], fa[S][0], acc[0][0]); SBAR(); acc[0][1] = MFMA32(fw[S][0], fa[S][1], acc[0][1]); SBAR(); \
                     acc[1][1] = MFMA32(fw[S][1], fa[S][1], acc[1][1]); SBAR(); acc[1][0] = MFMA32(fw[S][1], fa[S][0], acc[1][0]); SBAR(); } while (0)
        const float om0 = __uint_as_float(__builtin_amdgcn_raw_buffer_load_b32(rs, (32 * (aw & 1) + r32) * 4, WS_OM + tp * 256, 16));
        const float om1 = __uint_as_float(__builtin_amdgcn_raw_buffer_load_b32(rs, (32 * ((aw & 1) ^ 1) + r32) * 4, WS_OM + tp * 256, 16));
        LDSTEP(0); LDSTEP(1); LDSTEP(2); LDSTEP(3);
        __builtin_amdgcn_s_setprio(0);
        const f32x16 zero = {};
        acc[0][0] = MFMA32(fw[0][0], fa[0][0], zero); SBAR(); acc[0][1] = MFMA32(fw[0][0], fa[0][1], zero); SBAR();
        acc[1][1] = MFMA32(fw[0][1], fa[0][1], zero); SBAR(); acc[1][0] = MFMA32(fw[0][1], fa[0][0], zero); SBAR();
#pragma nounroll
        for (int it = 0; it < 6; ++it) { LDSTEP(0); MM4S(1); LDSTEP(1); MM4S(2); LDSTEP(2); MM4S(3); LDSTEP(3); MM4S(0); }
        SBAR(); MM4S(1); SBAR(); MM4S(2); SBAR(); MM4S(3);
        __builtin_amdgcn_s_setprio(2);
#pragma unroll
        for (int i = 0; i < 16; ++i) { acc[0][0][i] *= om0; acc[1][0][i] *= om0; acc[0][1][i] *= om1; acc[1][1][i] *= om1; }
    }
    const unsigned long long E = sy.nonce();
    {
        bf16x8 fw[4][2]; u32x4 fo[4][2];
#pragma unroll
        for (int s = 0; s < 4; ++s) { const int o = wbase + (KB_H + 8 * aw + 2 * s) * 2048; fw[s][0] = LD16S(wv0, o); fw[s][1] = LD16S(wv1, o); }
        sy.wait<16>(FL_OCNT, 4);
        sy.flags<4>((gu64*)(ws + WS_FO) + (2 * tp + hi) * 32 + 4 * aw + (lane & 3), E);
        SPIN(sp) {
            CBAR();
            unsigned m = 0;
#pragma unroll
            for (int s = 0; s < 4; ++s) {
                const int o = abase + (KB_H + 8 * aw + 2 * s) * 2048;
                fo[s][0] = __builtin_amdgcn_raw_buffer_load_b128(rs, av0, o, 16); fo[s][1] = __builtin_amdgcn_raw_buffer_load_b128(rs, av1, o, 16);
            }
#pragma unroll
            for (int s = 0; s < 8; ++s) m = max(max(m, fo[s >> 1][s & 1][0]), max(max(fo[s >> 1][s & 1][1], fo[s >> 1][s & 1][2]), fo[s >> 1][s & 1][3]));
            if (__all(m != ~0u)) break;
        }
#pragma unroll
        for (int s = 0; s < 4; ++s) {
            const bf16x8 a0 = __builtin_bit_cast(bf16x8, fo[s][0]), a1 = __builtin_bit_cast(bf16x8, fo[s][1]);
            acc[0][0] = MFMA32(fw[s][0], a0, acc[0][0]); acc[0][1] = MFMA32(fw[s][0], a1, acc[0][1]);
            acc[1][1] = MFMA32(fw[s][1], a1, acc[1][1]); acc[1][0] = MFMA32(fw[s][1], a0, acc[1][0]);
        }
    }
    bf16x8 fw[16];
#pragma unroll
    for (int s = 0; s < 16; ++s) fw[s] = LD16S(wv0, wbase + (KB_H + 2 * s + 1) * 2048);
    asm volatile("s_barrier" ::: "memory");
#pragma unroll
    for (int r = 1; r < 4; ++r) {
        LAS f32x4* dst = (LAS f32x4*)((lds_ptr)lds + (((aw ^ r) * 3 + r - 1) * 4096)) + lane;
        const f32x16 a = acc[r >> 1][r & 1];
#pragma unroll
        for (int g = 0; g < 4; ++g) dst[g * 64] = f32x4{a[4 * g], a[4 * g + 1], a[4 * g + 2], a[4 * g + 3]};
    }
    asm volatile("s_waitcnt lgkmcnt(0)\n\ts_barrier" ::: "memory");
    f32x16 oc = acc[0][0];
#pragma unroll
    for (int i = 0; i < 12; ++i) {
        const f32x4 p = ((const LAS f32x4*)((lds_ptr)lds + aw * 12288) + lane)[i * 64];
        oc[4 * (i & 3)] += p[0]; oc[4 * (i & 3) + 1] += p[1]; oc[4 * (i & 3) + 2] += p[2]; oc[4 * (i & 3) + 3] += p[3];
    }
    PIN(oc);
    {
        u32x4 fo[16];
        sy.flags<0>((gu64*)(ws + WS_FO) + (2 * tp + (aw & 1)) * 32 + 16 + (lane & 15), E);
        SPIN(sp) {
            CBAR();
            unsigned m = 0;
#pragma unroll
            for (int s = 0; s < 16; ++s) fo[s] = __builtin_amdgcn_raw_buffer_load_b128(rs, av0, abase + (KB_H + 2 * s + 1) * 2048, 16);
#pragma unroll
            for (int s = 0; s < 16; ++s) m = max(max(m, fo[s][0]), max(max(fo[s][1], fo[s][2]), fo[s][3]));
            if (__all(m != ~0u)) break;
        }
#pragma unroll
        for (int s = 0; s < 16; ++s) oc = MFMA32(fw[s], __builtin_bit_cast(bf16x8, fo[s]), oc);
    }
    {
        u16* dst = out + (size_t)(64 * tp + 32 * (aw & 1) + r32) * D_ + 64 * np + 32 * (aw >> 1) + 8 * hi;
#pragma unroll
        for (int g = 0; g < 4; g += 2) {
            const auto r0 = __builtin_amdgcn_permlane32_swap(cvtpk(oc[4 * g], oc[4 * g + 1]), cvtpk(oc[4 * g + 4], oc[4 * g + 5]), false, false);
            const auto r1 = __builtin_amdgcn_permlane32_swap(cvtpk(oc[4 * g + 2], oc[4 * g + 3]), cvtpk(oc[4 * g + 6], oc[4 * g + 7]), false, false);
            *(u32x4*)(dst + 8 * g) = u32x4{r0[0], r1[0], r0[1], r1[1]};
        }
    }
}

extern "C" void kernel_launch(void* const* d_in, const int* in_sizes, int n_in, void* d_out, int out_size, void* d_ws,
                              size_t ws_size, hipStream_t stream) {
    fused<<<256, 768, 0, stream>>>((const u16*)d_in[0], (const u16*)d_in[1], (const u16*)d_in[2], (const u16*)d_in[3], (const u16*)d_in[4],
                                   (const u16*)d_in[6], (const u16*)d_in[7], (const u16*)d_in[8], (const u16*)d_in[9], (char*)d_ws, (u16*)d_out);
}
```
